```python
import torch

def _softmax(x, axis=-1):
    return torch.softmax(x, dim=axis)
def _gelu(x):
    return torch.nn.functional.gelu(x, approximate="tanh")
def _silu(x):
    return torch.nn.functional.silu(x)
def _sigmoid(x):
    return torch.sigmoid(x)
def _layernorm(x, axis=-1, eps=1e-5):
    m = x.mean(dim=axis, keepdim=True)
    v = x.var(dim=axis, keepdim=True, unbiased=False)
    return (x - m) / torch.sqrt(v + eps)
def _scatter_add(tgt, idx, src, axis=0):
    return tgt.clone().index_add_(axis, idx, src)
def _segment_sum(src, seg, n):
    out = torch.zeros((n,) + src.shape[1:], dtype=src.dtype)
    return out.index_add_(0, seg, src)
def _pad(x, pw):
    flat = tuple(v for pair in reversed(pw) for v in pair)
    return torch.nn.functional.pad(x, flat)

kernel_name = 'parallel_sum__sliding_block_cached__glu_gated_bilinear__decode'

def setup_inputs():

    _w = (lambda a: (lambda t: t.to(torch.bfloat16) if t.is_floating_point() else t)(torch.as_tensor(a)))
    def _A_setup():
        B, Sq, D = 4, 1, 1024
        Hq, Hkv, Sk, Dh = 32, 4, 8192, 128
        assert Hq % Hkv == 0, (Hq, Hkv)
        return {
            "x": _w((lambda *s: torch.randn(*s, dtype=torch.bfloat16) * 0.05)(B, Sq, D)),
            "k_cache": _w((lambda *s: torch.randn(*s, dtype=torch.bfloat16) * 1.6)(B, Hkv, Sk, Dh)),
            "v_cache": _w((lambda *s: torch.randn(*s, dtype=torch.bfloat16) * 0.8)(B, Hkv, Sk, Dh)),
            "wq": _w((lambda *s: torch.randn(*s, dtype=torch.bfloat16) * 1.6)(D, Hq * Dh) * (D ** -0.5)),
            "wo": _w((lambda *s: torch.randn(*s, dtype=torch.bfloat16) * 0.8)(Hq * Dh, D) * ((Hq * Dh) ** -0.5)),
        }
    _A_in = _A_setup()

    def _B_setup():
        B, S, D, F = 4, 1, 1024, 3584
        return {
            "x": _w((lambda *s: torch.randn(*s, dtype=torch.bfloat16) * 0.05)(B, S, D)),
            "w_gate": _w((lambda *s: torch.randn(*s, dtype=torch.bfloat16) * 0.4)(D, F) * (D ** -0.5)),
            "w_up": _w((lambda *s: torch.randn(*s, dtype=torch.bfloat16) * 0.4)(D, F) * (D ** -0.5)),
            "w_down": _w((lambda *s: torch.randn(*s, dtype=torch.bfloat16) * 0.4)(F, D) * (F ** -0.5)),
        }
    _B_in = _B_setup()

    return {
        "A_x": _A_in["x"],
        "A_k_cache": _A_in["k_cache"],
        "A_v_cache": _A_in["v_cache"],
        "A_wq": _A_in["wq"],
        "A_wo": _A_in["wo"],
        "B_x": _B_in["x"],
        "B_w_gate": _B_in["w_gate"],
        "B_w_up": _B_in["w_up"],
        "B_w_down": _B_in["w_down"],
    }

def reference(A_x, A_k_cache, A_v_cache, A_wq, A_wo, B_x, B_w_gate, B_w_up, B_w_down):
    def _A_ref(x, k_cache, v_cache, wq, wo):
        B, Sq, D = x.shape
        Hq, Dh = 32, 128
        Hkv, Sk = k_cache.shape[1], k_cache.shape[2]
        W = min(4096, max(256, Sk // 4))
        rep = Hq // Hkv
        q = (lambda x, *d: x.permute(*d))((x @ wq).reshape(B, Sq, Hq, Dh), 0, 2, 1, 3)
        k = (lambda x, n, axis: torch.repeat_interleave(x, n, dim=axis))(k_cache[:, :, Sk - W:, :], rep, axis=1)
        v = (lambda x, n, axis: torch.repeat_interleave(x, n, dim=axis))(v_cache[:, :, Sk - W:, :], rep, axis=1)
        scores = torch.einsum("bhqd,bhkd->bhqk", q, k) * (Dh ** -0.5)
        p = _softmax(scores, axis=-1)
        o = torch.einsum("bhqk,bhkd->bhqd", p, v)
        return (lambda x, *d: x.permute(*d))(o, 0, 2, 1, 3).reshape(B, Sq, Hq * Dh) @ wo

    def _B_ref(x, w_gate, w_up, w_down):
        g = torch.einsum("bsd,df->bsf", x, w_gate)
        u = torch.einsum("bsd,df->bsf", x, w_up)
        h = g * u
        return torch.einsum("bsf,fd->bsd", h, w_down)

    x = A_x
    xn = _layernorm(x, axis=-1)
    return x + _A_ref(xn, A_k_cache, A_v_cache, A_wq, A_wo) + _B_ref(xn, B_w_gate, B_w_up, B_w_down)
```

```cpp
#include <hip/hip_runtime.h>
#include <hip/hip_bf16.h>
#include <stdint.h>

typedef unsigned v4u __attribute__((ext_vector_type(4)));
typedef unsigned short u16;

constexpr int NB = 4, DM = 1024, HQ = 32, HKV = 4, SK = 8192, DH = 128, WIN = 2048, FF = 3584;
constexpr int NSPLIT = 16, KPS = WIN / NSPLIT;

__device__ __forceinline__ float bf2f(unsigned b) { return __builtin_bit_cast(float, b << 16); }
__device__ __forceinline__ float bflo(unsigned w) { return __builtin_bit_cast(float, w << 16); }
__device__ __forceinline__ float bfhi(unsigned w) { return __builtin_bit_cast(float, w & 0xffff0000u); }
__device__ __forceinline__ unsigned f2bf(float f) { unsigned u = __builtin_bit_cast(unsigned, f); return (u + 0x7fffu + ((u >> 16) & 1u)) >> 16; }
__device__ __forceinline__ float rbf(float f) { return bf2f(f2bf(f)); }

__device__ void layernorm_to_lds(const u16* x, float* xn, float* red) {
  const int tid = threadIdx.x, w = tid >> 6, lane = tid & 63;
  float v[16];
  const v4u* p = (const v4u*)(x + w * DM);
  v4u a = p[lane], b = p[lane + 64];
  for (int i = 0; i < 4; ++i) { v[2 * i] = bflo(a[i]); v[2 * i + 1] = bfhi(a[i]); v[8 + 2 * i] = bflo(b[i]); v[8 + 2 * i + 1] = bfhi(b[i]); }
  float s = 0; for (int i = 0; i < 16; ++i) s += v[i];
  for (int o = 32; o; o >>= 1) s += __shfl_xor(s, o);
  const float mean = s * (1.f / DM);
  float q = 0; for (int i = 0; i < 16; ++i) { float d = v[i] - mean; q += d * d; }
  for (int o = 32; o; o >>= 1) q += __shfl_xor(q, o);
  const float var = rbf(q * (1.f / DM));
  const float mb = rbf(mean);
  const float sd = rbf(sqrtf(rbf(var + 1e-5f)));
  for (int i = 0; i < 8; ++i) { xn[w * DM + lane * 8 + i] = rbf(rbf(v[i] - mb) / sd); xn[w * DM + 512 + lane * 8 + i] = rbf(rbf(v[8 + i] - mb) / sd); }
  __syncthreads();
}

__global__ void __launch_bounds__(256) k_proj(const u16* x, const u16* wq, const u16* wg, const u16* wu, u16* qo, u16* ho) {
  __shared__ float xn[NB * DM];
  __shared__ float red[2][64][NB * 32 + 1];
  layernorm_to_lds(x, xn, nullptr);
  const int tid = threadIdx.x, c4 = tid & 3, rg = tid >> 2;
  const bool isq = blockIdx.x < 128;
  const int nmat = isq ? 1 : 2;
  const int col0 = isq ? blockIdx.x * 32 : (blockIdx.x - 128) * 32;
  const int ld = isq ? HQ * DH : FF;
  for (int mt = 0; mt < nmat; ++mt) {
    const u16* W = isq ? wq : (mt == 0 ? wg : wu);
    float acc[NB][8];
    for (int b = 0; b < NB; ++b) for (int j = 0; j < 8; ++j) acc[b][j] = 0.f;
    v4u wv[16];
#pragma unroll
    for (int i = 0; i < 16; ++i) wv[i] = __builtin_nontemporal_load((const v4u*)(W + (size_t)(rg + 64 * i) * ld + col0 + c4 * 8));
#pragma unroll
    for (int i = 0; i < 16; ++i) {
      const int k = rg + 64 * i;
      float wf[8];
      for (int j = 0; j < 4; ++j) { wf[2 * j] = bflo(wv[i][j]); wf[2 * j + 1] = bfhi(wv[i][j]); }
      for (int b = 0; b < NB; ++b) { const float xv = xn[b * DM + k]; for (int j = 0; j < 8; ++j) acc[b][j] = fmaf(xv, wf[j], acc[b][j]); }
    }
    for (int b = 0; b < NB; ++b) for (int j = 0; j < 8; ++j) red[mt][rg][b * 32 + c4 * 8 + j] = acc[b][j];
  }
  __syncthreads();
  if (tid < 128) {
    float s0 = 0, s1 = 0;
    for (int r = 0; r < 64; ++r) s0 += red[0][r][tid];
    if (!isq) for (int r = 0; r < 64; ++r) s1 += red[1][r][tid];
    const int b = tid >> 5, c = tid & 31;
    if (isq) qo[b * HQ * DH + col0 + c] = f2bf(s0);
    else ho[b * FF + col0 + c] = f2bf(rbf(s0) * rbf(s1));
  }
}

__global__ void __launch_bounds__(256) k_attn(const u16* q, const u16* kc, const u16* vc, float* po, float* pml) {
  __shared__ float qs[8][DH];
  __shared__ float sc[8][KPS];
  __shared__ float red[16][8 * DH];
  const int s = blockIdx.x, kvh = blockIdx.y, b = blockIdx.z, tid = threadIdx.x;
  for (int i = tid; i < 8 * DH; i += 256) qs[i >> 7][i & 127] = bf2f(q[b * HQ * DH + kvh * 8 * DH + i]);
  const size_t base = ((size_t)(b * HKV + kvh) * SK + (SK - WIN) + s * KPS) * DH;
  const int c = tid & 15, r = tid >> 4;
  v4u kv[8], vv[8];
#pragma unroll
  for (int i = 0; i < 8; ++i) kv[i] = __builtin_nontemporal_load((const v4u*)(kc + base + (size_t)(r + 16 * i) * DH + c * 8));
#pragma unroll
  for (int i = 0; i < 8; ++i) vv[i] = __builtin_nontemporal_load((const v4u*)(vc + base + (size_t)(r + 16 * i) * DH + c * 8));
  __syncthreads();
#pragma unroll
  for (int i = 0; i < 8; ++i) {
    float kf[8];
    for (int j = 0; j < 4; ++j) { kf[2 * j] = bflo(kv[i][j]); kf[2 * j + 1] = bfhi(kv[i][j]); }
    for (int h = 0; h < 8; ++h) {
      float d = 0; for (int j = 0; j < 8; ++j) d = fmaf(kf[j], qs[h][c * 8 + j], d);
      for (int o = 8; o; o >>= 1) d += __shfl_xor(d, o);
      if (c == 0) sc[h][r + 16 * i] = rbf(rbf(d) * 0.08838834764831845f);
    }
  }
  __syncthreads();
  {
    const int h = tid >> 5, l = tid & 31;
    float m = -INFINITY; for (int i = l; i < KPS; i += 32) m = fmaxf(m, sc[h][i]);
    for (int o = 16; o; o >>= 1) m = fmaxf(m, __shfl_xor(m, o));
    float sum = 0; for (int i = l; i < KPS; i += 32) { float p = __expf(sc[h][i] - m); sc[h][i] = p; sum += p; }
    for (int o = 16; o; o >>= 1) sum += __shfl_xor(sum, o);
    if (l == 0) { const int row = (b * HQ + kvh * 8 + h) * NSPLIT + s; pml[2 * row] = m; pml[2 * row + 1] = sum; }
  }
  __syncthreads();
  float acc[8][8];
  for (int h = 0; h < 8; ++h) for (int j = 0; j < 8; ++j) acc[h][j] = 0;
#pragma unroll
  for (int i = 0; i < 8; ++i) {
    float vf[8];
    for (int j = 0; j < 4; ++j) { vf[2 * j] = bflo(vv[i][j]); vf[2 * j + 1] = bfhi(vv[i][j]); }
    for (int h = 0; h < 8; ++h) { const float p = sc[h][r + 16 * i]; for (int j = 0; j < 8; ++j) acc[h][j] = fmaf(p, vf[j], acc[h][j]); }
  }
  for (int h = 0; h < 8; ++h) for (int j = 0; j < 8; ++j) red[r][h * DH + c * 8 + j] = acc[h][j];
  __syncthreads();
  for (int i = tid; i < 8 * DH; i += 256) {
    float t = 0; for (int rr = 0; rr < 16; ++rr) t += red[rr][i];
    const int h = i >> 7, d = i & 127;
    po[((size_t)(b * HQ + kvh * 8 + h) * NSPLIT + s) * DH + d] = t;
  }
}

__global__ void __launch_bounds__(256) k_out(const float* po, const float* pml, const u16* hh, const u16* wo, const u16* wd, float* part) {
  __shared__ float act[NB][256];
  __shared__ float red[16][NB * 128];
  const int rb = blockIdx.x >> 3, cb = blockIdx.x & 7, tid = threadIdx.x;
  const bool iso = rb < 16;
  const u16* W = iso ? wo + (size_t)rb * 256 * DM : wd + (size_t)(rb - 16) * 256 * DM;
  const int c = tid & 15, r = tid >> 4;
  v4u wv[16];
#pragma unroll
  for (int i = 0; i < 16; ++i) wv[i] = __builtin_nontemporal_load((const v4u*)(W + (size_t)(r + 16 * i) * DM + cb * 128 + c * 8));
  for (int b = 0; b < NB; ++b) {
    if (iso) {
      const int k = rb * 256 + tid, h = k >> 7, d = k & 127;
      const int row = (b * HQ + h) * NSPLIT;
      float M = -INFINITY; for (int s = 0; s < NSPLIT; ++s) M = fmaxf(M, pml[2 * (row + s)]);
      float L = 0, o = 0;
      for (int s = 0; s < NSPLIT; ++s) { const float w = __expf(pml[2 * (row + s)] - M); L += w * pml[2 * (row + s) + 1]; o += w * po[(size_t)(row + s) * DH + d]; }
      act[b][tid] = rbf(o / L);
    } else act[b][tid] = bf2f(hh[b * FF + (rb - 16) * 256 + tid]);
  }
  __syncthreads();
  float acc[NB][8];
  for (int b = 0; b < NB; ++b) for (int j = 0; j < 8; ++j) acc[b][j] = 0.f;
#pragma unroll
  for (int i = 0; i < 16; ++i) {
    float wf[8];
    for (int j = 0; j < 4; ++j) { wf[2 * j] = bflo(wv[i][j]); wf[2 * j + 1] = bfhi(wv[i][j]); }
    for (int b = 0; b < NB; ++b) { const float xv = act[b][r + 16 * i]; for (int j = 0; j < 8; ++j) acc[b][j] = fmaf(xv, wf[j], acc[b][j]); }
  }
  for (int b = 0; b < NB; ++b) for (int j = 0; j < 8; ++j) red[r][b * 128 + c * 8 + j] = acc[b][j];
  __syncthreads();
  for (int i = tid; i < NB * 128; i += 256) {
    float t = 0; for (int rr = 0; rr < 16; ++rr) t += red[rr][i];
    const int b = i >> 7, cc = i & 127;
    part[(size_t)rb * NB * DM + b * DM + cb * 128 + cc] = t;
  }
}

__global__ void __launch_bounds__(256) k_fin(const float* part, const u16* x, u16* out) {
  const int i = blockIdx.x * 256 + threadIdx.x;
  float a = 0, f = 0;
  for (int r = 0; r < 16; ++r) a += part[(size_t)r * NB * DM + i];
  for (int r = 16; r < 30; ++r) f += part[(size_t)r * NB * DM + i];
  out[i] = f2bf(rbf(bf2f(x[i]) + rbf(a)) + rbf(f));
}

extern "C" void kernel_launch(void* const* d_in, const int* in_sizes, int n_in, void* d_out, int out_size, void* d_ws, size_t ws_size, hipStream_t stream) {
  const u16* x = (const u16*)d_in[0]; const u16* kc = (const u16*)d_in[1]; const u16* vc = (const u16*)d_in[2];
  const u16* wq = (const u16*)d_in[3]; const u16* wo = (const u16*)d_in[4];
  const u16* wg = (const u16*)d_in[6]; const u16* wu = (const u16*)d_in[7]; const u16* wd = (const u16*)d_in[8];
  char* ws = (char*)d_ws;
  u16* q = (u16*)ws; u16* h = (u16*)(ws + 65536); float* pml = (float*)(ws + 131072); float* po = (float*)(ws + 262144); float* part = (float*)(ws + 262144 + 2097152);
  k_proj<<<240, 256, 0, stream>>>(x, wq, wg, wu, q, h);
  k_attn<<<dim3(NSPLIT, HKV, NB), 256, 0, stream>>>(q, kc, vc, po, pml);
  k_out<<<240, 256, 0, stream>>>(po, pml, h, wo, wd, part);
  k_fin<<<16, 256, 0, stream>>>(part, x, (u16*)d_out);
}
```

```cpp
#include <hip/hip_runtime.h>
#include <hip/hip_bf16.h>
#include <stdint.h>

#define STAMPS 0
#define WIN1 26
#define WIN2 26
#define NT_STR ""
#define P_GAP 5
#define DRY 0

typedef unsigned v4u __attribute__((ext_vector_type(4)));
typedef unsigned v2u __attribute__((ext_vector_type(2)));
typedef float f32x4 __attribute__((ext_vector_type(4)));
typedef short bf16x8 __attribute__((ext_vector_type(8)));
typedef short s16x4 __attribute__((ext_vector_type(4)));
typedef unsigned short u16;
#define LAS __attribute__((address_space(3)))
#define GAS __attribute__((address_space(1)))
typedef __amdgpu_buffer_rsrc_t rsrc_t;

constexpr int NTHR = 640;
constexpr int WS_E = 0;
constexpr int WS_MB = 4096, MB_Q = 0, MB_2 = 256, MB_O = 512, MB_G = 640, MB_5 = 768;
constexpr int WS_GQ = WS_MB + 256 * 1024;
constexpr int WS_GO = WS_GQ + 4 * 4 * 2048 * 8;
constexpr int WS_GGU = WS_GO + 4 * 2048 * 8;
constexpr int WS_G5 = WS_GGU + 4 * 4 * 1792 * 8;
constexpr int WS_G2 = WS_G5 + 16 * 16 * 16 * 16 * 8;
constexpr int WS_END = WS_G2 + 16 * 16 * 16 * 384;
constexpr int WS_STAMP = 8 << 20;
constexpr int NUNIT = 17;
constexpr int L_XN = NUNIT * 8192;
constexpr int L_QS = L_XN + 8192;
constexpr int L_PS = L_QS + 2048;
constexpr int L_OF = L_PS + 4096;
constexpr int L_HF = L_OF + 2112;
constexpr int L_ML = L_HF + 2112;
constexpr int L_CNT = L_ML + 512;
constexpr int L_BYTES = L_CNT + 64;
static_assert(L_BYTES <= 160 * 1024, "LDS");
enum { C_PROG = 0, C_XN = 1, C_TAGV = 2, C_TAG = 3, C_QS = 4, C_P = 5, C_PJ = 6, C_HF = 7, C_OF = 8, C_G5 = 9, C_G2A = 10, C_G2B = 11, C_KD = 12 };
constexpr unsigned ALL1 = 0x01010101u, ALL2 = 0x02020202u;
constexpr int SPIN_G = 1 << 14, SPIN_L = 1 << 20;
constexpr int AUX_SC1 = 16;

struct Params { const u16 *x, *kc, *vc, *wq, *wo, *wg, *wu, *wd; u16* out; char* ws; };

__device__ __forceinline__ rsrc_t mk(const void* p, int bytes) { return __builtin_amdgcn_make_buffer_rsrc((void*)p, 0, bytes, 0x00020000); }
__device__ __forceinline__ v4u ldg(rsrc_t r, int voff, int soff) { return __builtin_amdgcn_raw_buffer_load_b128(r, voff, soff, AUX_SC1); }
__device__ __forceinline__ v2u ldg2(rsrc_t r, int voff, int soff) { return __builtin_amdgcn_raw_buffer_load_b64(r, voff, soff, AUX_SC1); }
__device__ __forceinline__ void stg(rsrc_t r, int voff, v4u v) { __builtin_amdgcn_raw_buffer_store_b128(v, r, voff, 0, AUX_SC1); }
__device__ __forceinline__ void stg2(rsrc_t r, int voff, v2u v) { __builtin_amdgcn_raw_buffer_store_b64(v, r, voff, 0, AUX_SC1); }
__device__ __forceinline__ void stg1(rsrc_t r, int voff, unsigned v) { __builtin_amdgcn_raw_buffer_store_b32(v, r, voff, 0, AUX_SC1); }
constexpr int OOB = 0x7ffffff0;
__device__ __forceinline__ float asf(unsigned u) { return __builtin_bit_cast(float, u); }
__device__ __forceinline__ unsigned asu(float f) { return __builtin_bit_cast(unsigned, f); }
__device__ __forceinline__ float bflo(unsigned w) { return asf(w << 16); }
__device__ __forceinline__ float bfhi(unsigned w) { return asf(w & 0xffff0000u); }
typedef __bf16 bf2_t __attribute__((ext_vector_type(2)));
typedef float f2_t __attribute__((ext_vector_type(2)));
__device__ __forceinline__ unsigned pk(float lo, float hi) { f2_t f = {lo, hi}; return __builtin_bit_cast(unsigned, __builtin_convertvector(f, bf2_t)); }
__device__ __forceinline__ float rbf(float f) { return bflo(pk(f, f)); }
__device__ __forceinline__ f32x4 mfma(v4u a, v4u b, f32x4 c) { return __builtin_amdgcn_mfma_f32_16x16x32_bf16(__builtin_bit_cast(bf16x8, a), __builtin_bit_cast(bf16x8, b), c, 0, 0, 0); }
template <int CTRL> __device__ __forceinline__ float dpp(float x) { return asf(__builtin_amdgcn_mov_dpp((int)asu(x), CTRL, 0xf, 0xf, true)); }
constexpr int XOR1 = 0xB1, XOR2 = 0x4E, XOR7 = 0x141, ROR8 = 0x128;
__device__ __forceinline__ float vmax(float a, float b) { float r; asm("v_max_f32 %0, %1, %2" : "=v"(r) : "v"(a), "v"(b)); return r; }
__device__ __forceinline__ float xrow_max(float x) {
  auto s = __builtin_amdgcn_permlane16_swap(asu(x), asu(x), false, false); x = vmax(asf(s[0]), asf(s[1]));
  auto t = __builtin_amdgcn_permlane32_swap(asu(x), asu(x), false, false); return vmax(asf(t[0]), asf(t[1]));
}
__device__ __forceinline__ float xrow_sum(float x) {
  auto s = __builtin_amdgcn_permlane16_swap(asu(x), asu(x), false, false); x = asf(s[0]) + asf(s[1]);
  auto t = __builtin_amdgcn_permlane32_swap(asu(x), asu(x), false, false); return asf(t[0]) + asf(t[1]);
}
__device__ __forceinline__ float wave_sum(float x) { x += dpp<XOR1>(x); x += dpp<XOR2>(x); x += dpp<XOR7>(x); x += dpp<ROR8>(x); return xrow_sum(x); }
#define SB() __builtin_amdgcn_sched_barrier(0)
#define LGKM0() asm volatile("s_waitcnt lgkmcnt(0)" ::: "memory")
#define CBAR() asm volatile("" ::: "memory")
#define PINV(v) asm volatile("" : "+v"(v))
#define STR2(x) #x
#define STR(x) STR2(x)
constexpr f32x4 Z4 = {0.f, 0.f, 0.f, 0.f};

__device__ __forceinline__ void set_byte(int c, int i, unsigned v) {
  asm volatile("s_mov_b64 exec, 1\n\tds_write_b8 %0, %1\n\ts_mov_b64 exec, -1" :: "v"(L_CNT + c * 4 + i), "v"(v) : "memory");
}
__device__ __forceinline__ void set_word(int c, unsigned v) {
  asm volatile("s_mov_b64 exec, 1\n\tds_write_b32 %0, %1\n\ts_mov_b64 exec, -1" :: "v"(L_CNT + c * 4), "v"(v) : "memory");
}
__device__ __forceinline__ unsigned get_word(LAS char* L, int c) {
  const unsigned v = __builtin_amdgcn_readfirstlane(__hip_atomic_load((LAS unsigned*)(L + L_CNT) + c, __ATOMIC_RELAXED, __HIP_MEMORY_SCOPE_WORKGROUP));
  CBAR();
  return v;
}
template <int SLEEP> __device__ __forceinline__ void wait_eq(LAS char* L, int c, unsigned want, bool dry) {
#pragma unroll 1
  for (int i = 0; i < SPIN_L; ++i) {
    if (get_word(L, c) == want || dry) break;
    if (SLEEP) __builtin_amdgcn_s_sleep(SLEEP);
  }
}
template <int SLEEP> __device__ __forceinline__ void wait_prog(LAS char* L, unsigned& prog, int want, bool dry) {
  if (prog < (unsigned)want) {
#pragma unroll 1
    for (int i = 0; i < SPIN_L; ++i) {
      prog = get_word(L, C_PROG);
      if (prog >= (unsigned)want || dry) break;
      if (SLEEP) __builtin_amdgcn_s_sleep(SLEEP);
    }
  }
  CBAR();
}
__device__ __forceinline__ int unit_of(int k) { return (k >= NUNIT ? k - NUNIT : k) * 8192; }
__device__ __forceinline__ v4u trfrag(LAS char* a0, int d) {
  const s16x4 lo = __builtin_amdgcn_ds_read_tr16_b64_v4i16((LAS s16x4*)a0), hi = __builtin_amdgcn_ds_read_tr16_b64_v4i16((LAS s16x4*)(a0 + d));
  const v2u a = __builtin_bit_cast(v2u, lo), b = __builtin_bit_cast(v2u, hi);
  return v4u{a[0], a[1], b[0], b[1]};
}
__device__ __forceinline__ bool tagged(v4u v, unsigned tag) { return (v[1] == tag) & (v[3] == tag); }
__device__ __forceinline__ int poll_words(char* mb, int m, unsigned tag, int l) {
  GAS unsigned* p = (GAS unsigned*)mb + l;
  unsigned zero = 0; PINV(zero);
  int cnt = 0;
  if (l <= m) {
    unsigned a = __hip_atomic_fetch_add(p, zero, __ATOMIC_RELAXED, __HIP_MEMORY_SCOPE_AGENT), b;
    SB();
    __builtin_amdgcn_s_sleep(P_GAP);
#pragma unroll 1
    for (int it = 0; it < SPIN_G; ++it) {
      b = __hip_atomic_fetch_add(p, zero, __ATOMIC_RELAXED, __HIP_MEMORY_SCOPE_AGENT);
      SB();
      ++cnt;
      if (__all(a == tag)) break;
      a = __hip_atomic_fetch_add(p, zero, __ATOMIC_RELAXED, __HIP_MEMORY_SCOPE_AGENT);
      SB();
      ++cnt;
      if (__all(b == tag)) break;
    }
  }
  CBAR();
  return __builtin_amdgcn_readfirstlane(cnt);
}
__device__ __forceinline__ void issue4(rsrc_t rws, int off, int stride, v4u (&Q)[4]) {
#pragma unroll
  for (int t = 0; t < 4; ++t) Q[t] = ldg(rws, off, t * stride);
  SB();
}
__device__ __forceinline__ void check4(rsrc_t rws, int off, int stride, unsigned tag, v4u (&Q)[4]) {
#pragma unroll 1
  for (int it = 0; it < SPIN_G; ++it) {
    if (__all(tagged(Q[0], tag) & tagged(Q[1], tag) & tagged(Q[2], tag) & tagged(Q[3], tag))) break;
    CBAR();
    issue4(rws, off, stride, Q);
  }
}

#define ST(i) do {} while (0)
#define NP(v, e) (void)(e)

__global__ void __launch_bounds__(NTHR, 3) fused_decode(Params p) {
  __shared__ __attribute__((aligned(16))) char smem[L_BYTES];
  LAS char* L = (LAS char*)smem;
  const int tid = threadIdx.x, w = __builtin_amdgcn_readfirstlane(tid >> 6), l = tid & 63;
  const int c = blockIdx.x, x = c & 7, j = c >> 3;
  const int i16 = l & 15, g = l >> 4;
  ST(0);
  const int xp = x >> 1, xo = x & 1, t64 = xo * 32 + j;
  const int bb = 2 * xo + (j >> 4), sp = j & 15, pair = bb * 4 + xp;
  const int qks = c & 3, qcb = c >> 2;
  const int rb = 2 * x + (j >> 4), cb = sp;
  const bool big = xp < 2;
  const int rb0 = big ? 4 * xp : 8 + 3 * (xp - 2);
  const bool has_gu = big || j >= 8, has_dn = big || j < 16 || ((j >> 3) & 1) == xo;
  const int gt = big ? t64 : xo * 24 + j - 8, gsrc = gt & 3, gcb = rb0 * 4 + (gt >> 2);
  const int rbd = rb0 + (big ? (t64 >> 4) : (j < 16 ? xo : 2));
  const int k_wo = has_gu ? 20 : 12, k_dn = k_wo + 4;

  if (__builtin_expect(w == 9, 1)) {
    __builtin_amdgcn_s_setprio(3);
    int k = 0, unit = 0;
#define TILE(WN, r, v0, v1, v2, v3, sbase, istride) do { \
      constexpr int DD = (WN - 2) / 8, N0 = (WN - 2) % 8; \
      const rsrc_t r_ = (r); const int sb_ = (sbase), is_ = (istride); \
      _Pragma("unroll 1") for (int t = 0; t < 4; ++t) { \
        if (k >= NUNIT) { \
          if (k < NUNIT + 4) { _Pragma("unroll 1") for (int i = 0; i < SPIN_L; ++i) { const unsigned v = get_word(L, C_PJ); if ((v & 0x03030303u) && (v & 0xff) && (v & 0xff00) && (v & 0xff0000) && (v >> 24)) break; } } \
          else if (k < NUNIT + 8) wait_eq<1>(L, C_KD, ALL1, false); \
          else { wait_eq<1>(L, C_G2A, ALL1, false); wait_eq<1>(L, C_G2B, ALL1, false); } \
        } \
        const int so = sb_ + t * 8 * is_; \
        const unsigned m = (unsigned)(uintptr_t)L + unit * 8192; \
        _Pragma("unroll") for (int n = 0; n < 8; ++n) { \
          asm volatile("s_mov_b32 m0, %0\n\ts_nop 0\n\tbuffer_load_dwordx4 %1, %2, %3 offen" NT_STR " lds\n\ts_waitcnt vmcnt(" STR(WN - 1) ")" \
                       :: "s"(m + n * 1024), "v"((n & 3) == 0 ? (v0) : (n & 3) == 1 ? (v1) : (n & 3) == 2 ? (v2) : (v3)), "s"(r_), "s"(so + n * is_) : "memory"); \
          if (n == N0 && k > DD) set_word(C_PROG, (unsigned)(k - DD)); \
        } \
        if (k == 0) __builtin_amdgcn_s_barrier(); \
        ++k; if (++unit == NUNIT) unit = 0; \
      } } while (0)
    const int r8 = l >> 3, ce = ((l & 7) ^ (r8 & 2)) * 16, co = ce ^ 64;
    const int kvo = (pair * 8192 + 6144 + sp * 128) * 256;
    const int kb = g * 256 + ((i16 ^ g) * 16), vb = g * 256 + ((i16 ^ (2 * g)) * 16);
#pragma unroll 1
    for (int tl = 0; tl < 7; ++tl) {
      if ((tl == 3 || tl == 4) && !has_gu) continue;
      if (tl == 6 && !has_dn) continue;
      const u16* base = tl == 0 ? p.wq : tl == 1 ? p.kc : tl == 2 ? p.vc : tl < 5 ? ((gsrc & 2) ? p.wu : p.wg) : tl == 5 ? p.wo : p.wd;
      const int bytes = tl == 0 || tl == 5 ? 8 << 20 : tl < 3 ? 32 << 20 : 7 << 20;
      const int ld = tl == 0 ? 8192 : tl < 3 ? 128 : tl < 5 ? 7168 : 2048;
      const int sb = tl == 0 ? qks * 256 * 8192 + qcb * 128 : tl < 3 ? kvo : tl < 5 ? ((gsrc & 1) * 512 + (tl - 3) * 256) * 7168 + gcb * 128
                   : (tl == 5 ? rb : rbd) * 256 * 2048 + cb * 128;
      const bool kv = tl == 1 || tl == 2;
      const int v0 = tl == 1 ? kb : tl == 2 ? vb : r8 * ld + ce;
      const int x1 = tl == 1 ? 64 : tl == 2 ? 128 : 64, x2 = tl == 1 ? 128 : 0;
      (void)kv; (void)co;
      if (WIN1 == WIN2 || tl < 5) TILE(WIN1, mk(base, bytes), v0, v0 ^ x1, v0 ^ x2, v0 ^ x1 ^ x2, sb, 8 * ld);
      else TILE(WIN2, mk(base, bytes), v0, v0 ^ x1, v0 ^ x2, v0 ^ x1 ^ x2, sb, 8 * ld);
    }
    if ((WIN2 - 2) / 8 >= 3) { asm volatile("s_waitcnt vmcnt(24)" ::: "memory"); set_word(C_PROG, (unsigned)(k - 3)); }
    if ((WIN2 - 2) / 8 >= 2) { asm volatile("s_waitcnt vmcnt(16)" ::: "memory"); set_word(C_PROG, (unsigned)(k - 2)); }
    asm volatile("s_waitcnt vmcnt(8)" ::: "memory"); set_word(C_PROG, (unsigned)(k - 1));
    asm volatile("s_waitcnt vmcnt(0)" ::: "memory"); set_word(C_PROG, (unsigned)k);
    ST(8);
  } else if (w < 8) {
    const rsrc_t rws = mk(p.ws, WS_END);
    char* mb = p.ws + WS_MB + c * 1024;
    const int w4 = w & 3, q4 = i16 >> 2, pp = l & 3;
    const int wa = (8 * g + q4) * 128 + (((2 * w4 + (pp >> 1)) ^ (2 * (((q4 >> 1) & 1) | ((g & 1) << 1)))) * 16) + 8 * (pp & 1);
    unsigned tag = 0, prog = 0;
    if (w >= 4) {
      const rsrc_t r = mk(p.x, 4 * 1024 * 2);
      const v4u a = __builtin_amdgcn_raw_buffer_load_b128(r, w4 * 2048 + l * 16, 0, 0), b = __builtin_amdgcn_raw_buffer_load_b128(r, w4 * 2048 + 1024 + l * 16, 0, 0);
      SB();
      __builtin_amdgcn_s_barrier();
      CBAR();
      SB();
      float v[16];
#pragma unroll
      for (int n = 0; n < 4; ++n) { v[2 * n] = bflo(a[n]); v[2 * n + 1] = bfhi(a[n]); v[8 + 2 * n] = bflo(b[n]); v[9 + 2 * n] = bfhi(b[n]); }
      float s = 0.f;
#pragma unroll
      for (int n = 0; n < 16; ++n) s += v[n];
      const float mean = wave_sum(s) * (1.f / 1024.f);
      float q = 0.f;
#pragma unroll
      for (int n = 0; n < 16; ++n) { const float d = v[n] - mean; q = fmaf(d, d, q); }
      const float var = rbf(wave_sum(q) * (1.f / 1024.f));
      const float mb_ = rbf(mean);
      const float rs = __builtin_amdgcn_rcpf(rbf(__builtin_amdgcn_sqrtf(rbf(var + 1e-5f))));
      v4u oa, ob;
#pragma unroll
      for (int n = 0; n < 4; ++n) {
        const unsigned da = pk(v[2 * n] - mb_, v[2 * n + 1] - mb_), db = pk(v[8 + 2 * n] - mb_, v[9 + 2 * n] - mb_);
        oa[n] = pk(bflo(da) * rs, bfhi(da) * rs); ob[n] = pk(bflo(db) * rs, bfhi(db) * rs);
      }
      *(LAS v4u*)(L + L_XN + w4 * 2048 + l * 16) = oa;
      *(LAS v4u*)(L + L_XN + w4 * 2048 + 1024 + l * 16) = ob;
      set_byte(C_XN, w4, 1u);
      ST(1);
    } else {
      __builtin_amdgcn_s_barrier();
      CBAR();
    }
#pragma unroll 1
    for (int pass = 0; pass < 2; ++pass) {
      int dryi = DRY ? 1 - pass : 0; asm volatile("" : "+s"(dryi));
      const bool dry = dryi != 0;
      if (!DRY && pass == 0 && w < 4) continue;
      const unsigned one = dry ? 0u : 1u;
      if (w < 4) {
        wait_eq<2>(L, C_TAGV, 1u, dry);
        tag = get_word(L, C_TAG);
        const int qoff = WS_GQ + (bb * 2048 + xp * 512 + w * 128 + l * 2) * 8;
        const int h = 2 * w + (l >> 5);
        int qw = L_QS + h * 256 + ((((l & 31) >> 1) ^ (2 * h)) * 16) + (l & 1) * 8;
        int qr = L_QS + (i16 & 7) * 256 + ((g ^ (2 * (i16 & 7))) * 16);
        int ka = (4 + w) * 8192 + i16 * 256 + ((g ^ i16) * 16);
        PINV(qw); PINV(qr); PINV(ka);
        wait_prog<2>(L, prog, 4, dry);
        ST(1);
        v4u Q[4] = {};
        if (!dry) { NP(np0, poll_words(mb + MB_Q + w * 64, 15, tag, l)); ST(2); issue4(rws, qoff, 4 * 2048 * 8, Q); }
        wait_prog<1>(L, prog, 5 + w, dry);
        v4u KF[8];
#pragma unroll
        for (int n = 0; n < 8; ++n) KF[n] = *(LAS v4u*)(L + ((ka ^ ((n & 3) * 64)) + (n >> 2) * 4096));
        set_byte(C_KD, w, one);
        SB();
        if (!dry) check4(rws, qoff, 4 * 2048 * 8, tag, Q);
        ST(3);
        const float a0 = (bflo(Q[0][0]) + bflo(Q[1][0])) + (bflo(Q[2][0]) + bflo(Q[3][0]));
        const float a1 = (bfhi(Q[0][0]) + bfhi(Q[1][0])) + (bfhi(Q[2][0]) + bfhi(Q[3][0]));
        const float a2 = (bflo(Q[0][2]) + bflo(Q[1][2])) + (bflo(Q[2][2]) + bflo(Q[3][2]));
        const float a3 = (bfhi(Q[0][2]) + bfhi(Q[1][2])) + (bfhi(Q[2][2]) + bfhi(Q[3][2]));
        *(LAS v2u*)(L + qw) = v2u{pk(a0, a1), pk(a2, a3)};
        set_byte(C_QS, w, one);
        wait_eq<0>(L, C_QS, ALL1, dry);
        v4u qf[4];
#pragma unroll
        for (int s = 0; s < 4; ++s) qf[s] = *(LAS v4u*)(L + (qr ^ (s * 64)));
        f32x4 d0 = Z4, d1 = Z4;
#pragma unroll
        for (int s = 0; s < 4; ++s) { d0 = mfma(KF[s], qf[s], d0); d1 = mfma(KF[4 + s], qf[s], d1); }
        float sc[8] = {d0[0], d0[1], d0[2], d0[3], d1[0], d1[1], d1[2], d1[3]};
#pragma unroll
        for (int n = 0; n < 8; n += 2) {
          const unsigned a = pk(sc[n], sc[n + 1]);
          const unsigned b = pk(bflo(a) * 0.08838834764831845f, bfhi(a) * 0.08838834764831845f);
          sc[n] = bflo(b); sc[n + 1] = bfhi(b);
        }
        float m = __builtin_fmaxf(__builtin_fmaxf(__builtin_fmaxf(sc[0], sc[1]), sc[2]), __builtin_fmaxf(__builtin_fmaxf(sc[3], sc[4]), sc[5]));
        m = __builtin_fmaxf(__builtin_fmaxf(m, sc[6]), sc[7]);
        m = xrow_max(m) * 1.4426950408889634f;
#pragma unroll
        for (int n = 0; n < 8; ++n) sc[n] = __builtin_amdgcn_exp2f(fmaf(sc[n], 1.4426950408889634f, -m));
        *(LAS v4u*)(L + L_PS + w * 1024 + l * 16) = v4u{pk(sc[0], sc[1]), pk(sc[2], sc[3]), pk(sc[4], sc[5]), pk(sc[6], sc[7])};
        const float ls = xrow_sum(((sc[0] + sc[1]) + (sc[2] + sc[3])) + ((sc[4] + sc[5]) + (sc[6] + sc[7])));
        *(LAS f2_t*)(L + L_ML + i16 * 32 + w * 8) = f2_t{m, ls};
        set_byte(C_P, w, one);
        ST(4);
      }
      if (w < 4 || pass == 1 || true) {
      }
      if (w >= 4 && pass == 0) {
      }
      if (w < 4 || pass == 1) {
        const int rr = 4 * g + q4;
        int va = 8 * 8192 + rr * 256 + (((2 * w + (pp >> 1)) ^ (2 * (rr & 7))) * 16) + 8 * (pp & 1);
        int goff = i16 < 8 && !dry ? WS_G2 + ((pair * 16 + 2 * i16 + (w >> 2)) * 16 + sp) * 384 + w4 * 64 + g * 16 : OOB;
        int loff = l < 8 && w4 == 0 && !dry ? goff + 256 : OOB;
        int noff = l < 8 && !dry ? WS_MB + (((j & 16) + 2 * l + (w >> 2)) * 8 + x) * 1024 + MB_2 + (sp * 4 + w4) * 4 : OOB;
        PINV(va); PINV(goff); PINV(loff); PINV(noff);
        v4u VF[4];
#pragma unroll
        for (int t = 0; t < 3; ++t) { wait_prog<1>(L, prog, 9 + t, dry); VF[t] = trfrag(L + va + t * 8192, 4096); }
        wait_eq<0>(L, C_P, ALL1, dry);
        v4u PF[4];
#pragma unroll
        for (int t = 0; t < 4; ++t) PF[t] = *(LAS v4u*)(L + L_PS + t * 1024 + l * 16);
        const f32x4 ml0 = *(LAS f32x4*)(L + L_ML + i16 * 32), ml1 = *(LAS f32x4*)(L + L_ML + i16 * 32 + 16);
        const f32x4 A0 = mfma(VF[0], PF[0], Z4), A1 = mfma(VF[1], PF[1], Z4), A2 = mfma(VF[2], PF[2], Z4);
        const float M = vmax(vmax(ml0[0], ml0[2]), vmax(ml1[0], ml1[2]));
        float a0 = __builtin_amdgcn_exp2f(ml0[0] - M), a1 = __builtin_amdgcn_exp2f(ml0[2] - M), a2 = __builtin_amdgcn_exp2f(ml1[0] - M), a3 = __builtin_amdgcn_exp2f(ml1[2] - M);
        const float Ls = (a0 * ml0[1] + a1 * ml0[3]) + (a2 * ml1[1] + a3 * ml1[3]);
        const float inv = __builtin_amdgcn_rcpf(Ls);
        float lse = M + __builtin_amdgcn_logf(Ls);
        f32x4 ov = (A0 * a0 + A1 * a1 + A2 * a2) * inv;
        a3 *= inv;
        PINV(ov); PINV(a3); PINV(lse);
        ST(5);
        wait_prog<0>(L, prog, 12, dry);
        ov += mfma(trfrag(L + va + 3 * 8192, 4096), PF[3], Z4) * a3;
        stg(rws, goff, v4u{pk(ov[0], ov[1]), tag, pk(ov[2], ov[3]), tag});
        stg2(rws, loff, v2u{asu(lse), tag});
        stg1(rws, noff, tag);
        set_byte(C_G2A + (w >> 2), w4, one);
        ST(6);
      }
      if (w >= 4) {
        const bool job = pass == 1;
        if (!job) { wait_eq<1>(L, C_XN, ALL1, false); wait_eq<1>(L, C_TAGV, 1u, false); tag = get_word(L, C_TAG); }
        if (!job || has_gu) {
          const int k0 = job ? 12 : 0, nt = job ? 8 : 4;
          LAS char* xa = L + L_XN + pp * 2048 + ((job ? (gsrc & 1) * 512 : qks * 256) + g * 8) * 2;
          int soff = i16 >= 4 ? OOB : job ? WS_GGU + ((gsrc * 4 + pp) * 1792 + gcb * 32 + w4 * 8 + g * 2) * 8 : WS_GQ + ((qks * 4 + pp) * 2048 + qcb * 32 + w4 * 8 + g * 2) * 8;
          const int rl = gt >> 4;
          const int gj = big ? (rl & 1) * 16 + l : (rl < 2 ? l : 16 + l), gx = big ? rl >> 1 : (rl < 2 ? rl : (l >> 3) & 1);
          int noff = job ? (l < 16 ? WS_MB + (gj * 8 + 2 * xp + gx) * 1024 + MB_G + (gt & 15) * 4 : OOB)
                         : WS_MB + ((l & 31) * 8 + 2 * (qcb >> 4) + (l >> 5)) * 1024 + MB_Q + ((qcb & 15) * 4 + qks) * 4;
          PINV(soff); PINV(noff);
          f32x4 acc0 = Z4, acc1 = Z4;
#pragma unroll 1
          for (int t = 0; t < nt; ++t) {
            const v4u x0 = *(LAS v4u*)(xa + t * 128), x1 = *(LAS v4u*)(xa + t * 128 + 64);
            LAS char* u = L + unit_of(k0 + t) + wa;
            wait_prog<1>(L, prog, k0 + t + 1, false);
            acc0 = mfma(trfrag(u, 512), x0, acc0);
            acc1 = mfma(trfrag(u + 4096, 512), x1, acc1);
          }
          if (job) ST(7); else ST(2);
          acc0 += acc1;
          stg(rws, soff, v4u{pk(acc0[0], acc0[1]), tag, pk(acc0[2], acc0[3]), tag});
          set_byte(C_PJ, w4, job ? 2u : 1u);
          if (w4 == 0) { wait_eq<0>(L, C_PJ, job ? ALL2 : ALL1, false); stg1(rws, noff, tag); }
          if (job) ST(8); else ST(3);
        } else wait_prog<4>(L, prog, k_dn + 4, false);
        if (has_dn) {
          const int hoff = WS_GGU + (g * 1792 + rbd * 128 + w4 * 32 + i16 * 2) * 8;
          v4u H[4] = {};
          if (job) {
            NP(np0, poll_words(mb + MB_G + w4 * 16, 3, tag, l));
            ST(9);
            issue4(rws, hoff, 4 * 1792 * 8, H);
            check4(rws, hoff, 4 * 1792 * 8, tag, H);
            ST(10);
          }
          const unsigned g0 = pk(bflo(H[0][0]) + bflo(H[1][0]), bfhi(H[0][0]) + bfhi(H[1][0])), g1 = pk(bflo(H[0][2]) + bflo(H[1][2]), bfhi(H[0][2]) + bfhi(H[1][2]));
          const unsigned u0 = pk(bflo(H[2][0]) + bflo(H[3][0]), bfhi(H[2][0]) + bfhi(H[3][0])), u1 = pk(bflo(H[2][2]) + bflo(H[3][2]), bfhi(H[2][2]) + bfhi(H[3][2]));
          *(LAS v2u*)(L + L_HF + g * 528 + w4 * 128 + i16 * 8) = v2u{pk(bflo(g0) * bflo(u0), bfhi(g0) * bfhi(u0)), pk(bflo(g1) * bflo(u1), bfhi(g1) * bfhi(u1))};
          set_byte(C_HF, w4, job ? 1u : 0u);
        }
      } else {
        int aa = pp * 528 + g * 16;
        const bool u1 = i16 & 4;
        int soff = i16 < 8 && !dry ? WS_G5 + ((cb * 16 + pp * 4 + w) * 16 + rb) * 128 + g * 32 + (u1 ? 16 : 0) : OOB;
        int noff = l < 4 && !dry ? WS_MB + (((w & 1) * 16 + cb) * 8 + 2 * l + (w >> 1)) * 1024 + MB_5 + rb * 4 : OOB;
        PINV(aa); PINV(soff); PINV(noff);
        v4u WF[8], DF[8];
#pragma unroll
        for (int t = 0; t < 4; ++t) {
          LAS char* u = L + unit_of(k_wo + t) + wa;
          wait_prog<1>(L, prog, k_wo + t + 1, dry);
          WF[2 * t] = trfrag(u, 512); WF[2 * t + 1] = trfrag(u + 4096, 512);
        }
        if (has_dn) {
#pragma unroll
          for (int t = 0; t < 3; ++t) {
            LAS char* u = L + unit_of(k_dn + t) + wa;
            wait_prog<1>(L, prog, k_dn + t + 1, dry);
            DF[2 * t] = trfrag(u, 512); DF[2 * t + 1] = trfrag(u + 4096, 512);
          }
        }
        ST(7);
        f32x4 acc0 = Z4, acc1 = Z4;
        wait_eq<0>(L, C_OF, 1u, dry);
#pragma unroll
        for (int s = 0; s < 8; s += 2) { acc0 = mfma(WF[s], *(LAS v4u*)(L + L_OF + aa + s * 64), acc0); acc1 = mfma(WF[s + 1], *(LAS v4u*)(L + L_OF + aa + s * 64 + 64), acc1); }
        ST(8);
        if (has_dn) {
          LAS char* u = L + unit_of(k_dn + 3) + wa;
          wait_eq<0>(L, C_HF, ALL1, dry);
          v4u hf[8];
#pragma unroll
          for (int s = 0; s < 8; ++s) hf[s] = *(LAS v4u*)(L + L_HF + aa + s * 64);
#pragma unroll
          for (int s = 0; s < 6; s += 2) { acc0 = mfma(DF[s], hf[s], acc0); acc1 = mfma(DF[s + 1], hf[s + 1], acc1); }
          ST(9);
          wait_prog<0>(L, prog, k_dn + 4, dry);
          acc0 = mfma(trfrag(u, 512), hf[6], acc0);
          acc1 = mfma(trfrag(u + 4096, 512), hf[7], acc1);
        }
        acc0 += acc1;
        stg(rws, soff, v4u{asu(u1 ? acc0[2] : acc0[0]), tag, asu(u1 ? acc0[3] : acc0[1]), tag});
        stg1(rws, noff, tag);
        set_byte(C_G5, w, one);
        ST(10);
      }
    }
  } else {
    __builtin_amdgcn_s_setprio(2);
    const rsrc_t rws = mk(p.ws, WS_END);
    char* mb = p.ws + WS_MB + c * 1024;
    const unsigned ev = __builtin_amdgcn_raw_buffer_load_b32(rws, WS_E, 0, AUX_SC1);
    const unsigned warm = __builtin_amdgcn_raw_buffer_load_b32(rws, WS_END - 4, 0, AUX_SC1);
    const int oidx = (rb >> 2) * 1024 + cb * 64 + (rb & 3) * 16 + (l & 7) * 2;
    const unsigned xres = *(const unsigned*)(p.x + oidx);
    if (l < 16) *((LAS unsigned*)(L + L_CNT) + l) = 0u;
    LGKM0();
    SB();
    __builtin_amdgcn_s_barrier();
    CBAR();
    SB();
    const unsigned tag = __builtin_amdgcn_readfirstlane(ev) + 1u;
    set_word(C_TAG, tag);
    set_word(C_TAGV, 1u);
    asm volatile("" :: "v"(warm));
    const rsrc_t rout = mk(p.out, 8192);
#pragma unroll 1
    for (int pass = DRY ? 0 : 1; pass < 2; ++pass) {
      int dryi = 1 - pass; asm volatile("" : "+s"(dryi));
      const bool dry = dryi != 0;
      const int off2 = WS_G2 + ((pair * 16 + sp) * 16 + g) * 384;
      int soff = l < 16 && !dry ? WS_GO + (bb * 2048 + xp * 512 + sp * 32 + l * 2) * 8 : OOB;
      int noff = l < 16 && !dry ? WS_MB + ((((sp >> 2) & 1) * 16 + l) * 8 + 2 * xp + (sp >> 3)) * 1024 + MB_O + (bb * 4 + (sp & 3)) * 4 : OOB;
      int ooff = l < 8 && !dry ? oidx * 2 : OOB;
      PINV(soff); PINV(noff); PINV(ooff);
      wait_eq<2>(L, C_G2A, ALL1, dry);
      ST(1);
      v4u P[4] = {}; v2u S[4] = {};
      if (!dry) {
        NP(np0, poll_words(mb + MB_2, 63, tag, l));
        ST(2);
#pragma unroll 1
        for (int it = 0; it < SPIN_G; ++it) {
          CBAR();
          bool ok = true;
#pragma unroll
          for (int t = 0; t < 4; ++t) { P[t] = ldg(rws, off2 + i16 * 16, t * 4 * 384); S[t] = ldg2(rws, off2 + 256, t * 4 * 384); }
#pragma unroll
          for (int t = 0; t < 4; ++t) ok &= tagged(P[t], tag) & (S[t][1] == tag);
          if (__all(ok)) break;
        }
      }
      ST(3);
      const float M = xrow_max(vmax(vmax(asf(S[0][0]), asf(S[1][0])), vmax(asf(S[2][0]), asf(S[3][0]))));
      float ws = 0.f, o0 = 0.f, o1 = 0.f, o2 = 0.f, o3 = 0.f;
#pragma unroll
      for (int t = 0; t < 4; ++t) {
        const float a = __builtin_amdgcn_exp2f(asf(S[t][0]) - M);
        ws += a; o0 = fmaf(a, bflo(P[t][0]), o0); o1 = fmaf(a, bfhi(P[t][0]), o1); o2 = fmaf(a, bflo(P[t][2]), o2); o3 = fmaf(a, bfhi(P[t][2]), o3);
      }
      const float inv = __builtin_amdgcn_rcpf(xrow_sum(ws));
      o0 = xrow_sum(o0) * inv; o1 = xrow_sum(o1) * inv; o2 = xrow_sum(o2) * inv; o3 = xrow_sum(o3) * inv;
      stg(rws, soff, v4u{pk(o0, o1), tag, pk(o2, o3), tag});
      stg1(rws, noff, tag);
      ST(4);
      v4u O[4] = {};
      if (!dry) {
        NP(np1, poll_words(mb + MB_O, 15, tag, l));
        ST(5);
        issue4(rws, WS_GO + (rb * 128 + l * 2) * 8, 2048 * 8, O);
        check4(rws, WS_GO + (rb * 128 + l * 2) * 8, 2048 * 8, tag, O);
      }
#pragma unroll
      for (int t = 0; t < 4; ++t) *(LAS v2u*)(L + L_OF + t * 528 + l * 8) = v2u{O[t][0], O[t][2]};
      set_word(C_OF, dry ? 0u : 1u);
      ST(6);
      wait_eq<1>(L, C_G5, ALL1, dry);
      ST(7);
      v4u P0 = {}, P1 = {};
      if (!dry) {
        NP(np2, poll_words(mb + MB_5, 15, tag, l));
        ST(8);
        const int off = WS_G5 + ((cb * 16 + rb) * 256 + l * 2) * 8;
#pragma unroll 1
        for (int it = 0; it < SPIN_G; ++it) {
          CBAR();
          P0 = ldg(rws, off, 0); P1 = ldg(rws, off, 1024);
          if (__all(tagged(P0, tag) & tagged(P1, tag))) break;
        }
      }
      ST(9);
      float a = asf(P0[0]) + asf(P1[0]), b = asf(P0[2]) + asf(P1[2]);
      a += dpp<ROR8>(a); b += dpp<ROR8>(b);
      a = xrow_sum(a); b = xrow_sum(b);
      __builtin_amdgcn_raw_buffer_store_b32(pk(bflo(xres) + a, bfhi(xres) + b), rout, ooff, 0, 0);
      stg1(rws, c == 0 && l == 0 && !dry ? WS_E : OOB, tag);
      ST(10);
    }
  }
}

extern "C" void kernel_launch(void* const* d_in, const int* in_sizes, int n_in, void* d_out, int out_size, void* d_ws, size_t ws_size, hipStream_t stream) {
  Params p;
  p.x = (const u16*)d_in[0]; p.kc = (const u16*)d_in[1]; p.vc = (const u16*)d_in[2]; p.wq = (const u16*)d_in[3]; p.wo = (const u16*)d_in[4];
  p.wg = (const u16*)d_in[6]; p.wu = (const u16*)d_in[7]; p.wd = (const u16*)d_in[8]; p.out = (u16*)d_out; p.ws = (char*)d_ws;
  fused_decode<<<256, NTHR, 0, stream>>>(p);
}
```

```cpp
#define LEAK_I 0
#define LEAK_W0 9
#define LEAK_W1 9
#define DRY 0
#define LEAK_X 1
#include <hip/hip_runtime.h>
#include <hip/hip_bf16.h>
#include <stdint.h>

#define STAMPS 0
#define WIN1 26
#define WIN2 26
#define NT_STR " nt"
#define P_GAP 5

typedef unsigned v4u __attribute__((ext_vector_type(4)));
typedef unsigned v2u __attribute__((ext_vector_type(2)));
typedef float f32x4 __attribute__((ext_vector_type(4)));
typedef short bf16x8 __attribute__((ext_vector_type(8)));
typedef short s16x4 __attribute__((ext_vector_type(4)));
typedef unsigned short u16;
#define LAS __attribute__((address_space(3)))
#define GAS __attribute__((address_space(1)))
typedef __amdgpu_buffer_rsrc_t rsrc_t;

constexpr int NTHR = 640;
constexpr int WS_E = 0;
constexpr int WS_MB = 4096, MB_Q = 0, MB_2 = 256, MB_O = 512, MB_G = 640, MB_5 = 768;
constexpr int WS_GQ = WS_MB + 256 * 1024;
constexpr int WS_GO = WS_GQ + 4 * 4 * 2048 * 8;
constexpr int WS_GGU = WS_GO + 4 * 2048 * 8;
constexpr int WS_G5 = WS_GGU + 4 * 4 * 1792 * 8;
constexpr int WS_G2 = WS_G5 + 16 * 16 * 16 * 16 * 8;
constexpr int WS_END = WS_G2 + 16 * 16 * 16 * 384;
constexpr int WS_STAMP = 8 << 20;
constexpr int NUNIT = 17;
constexpr int L_XN = NUNIT * 8192;
constexpr int L_QS = L_XN + 8192;
constexpr int L_PS = L_QS + 2048;
constexpr int L_OF = L_PS + 4096;
constexpr int L_HF = L_OF + 2112;
constexpr int L_ML = L_HF + 2112;
constexpr int L_CNT = L_ML + 512;
constexpr int L_BYTES = L_CNT + 64;
static_assert(L_BYTES <= 160 * 1024, "LDS");
enum { C_PROG = 0, C_XN = 1, C_TAGV = 2, C_TAG = 3, C_QS = 4, C_P = 5, C_PJ = 6, C_HF = 7, C_OF = 8, C_G5 = 9, C_G2A = 10, C_G2B = 11, C_KD = 12 };
constexpr unsigned ALL1 = 0x01010101u, ALL2 = 0x02020202u;
constexpr int SPIN_G = 1 << 14, SPIN_L = 1 << 20;
constexpr int AUX_SC1 = 16;

struct Params { const u16 *x, *kc, *vc, *wq, *wo, *wg, *wu, *wd; u16* out; char* ws; };

__device__ __forceinline__ rsrc_t mk(const void* p, int bytes) { return __builtin_amdgcn_make_buffer_rsrc((void*)p, 0, bytes, 0x00020000); }
__device__ __forceinline__ v4u ldg(rsrc_t r, int voff, int soff) { return __builtin_amdgcn_raw_buffer_load_b128(r, voff, soff, AUX_SC1); }
__device__ __forceinline__ v2u ldg2(rsrc_t r, int voff, int soff) { return __builtin_amdgcn_raw_buffer_load_b64(r, voff, soff, AUX_SC1); }
__device__ __forceinline__ void stg(rsrc_t r, int voff, v4u v) { __builtin_amdgcn_raw_buffer_store_b128(v, r, voff, 0, AUX_SC1); }
__device__ __forceinline__ void stg2(rsrc_t r, int voff, v2u v) { __builtin_amdgcn_raw_buffer_store_b64(v, r, voff, 0, AUX_SC1); }
__device__ __forceinline__ void stg1(rsrc_t r, int voff, unsigned v) { __builtin_amdgcn_raw_buffer_store_b32(v, r, voff, 0, AUX_SC1); }
constexpr int OOB = 0x7ffffff0;
__device__ __forceinline__ float asf(unsigned u) { return __builtin_bit_cast(float, u); }
__device__ __forceinline__ unsigned asu(float f) { return __builtin_bit_cast(unsigned, f); }
__device__ __forceinline__ float bflo(unsigned w) { return asf(w << 16); }
__device__ __forceinline__ float bfhi(unsigned w) { return asf(w & 0xffff0000u); }
typedef __bf16 bf2_t __attribute__((ext_vector_type(2)));
typedef float f2_t __attribute__((ext_vector_type(2)));
__device__ __forceinline__ unsigned pk(float lo, float hi) { f2_t f = {lo, hi}; return __builtin_bit_cast(unsigned, __builtin_convertvector(f, bf2_t)); }
__device__ __forceinline__ float rbf(float f) { return bflo(pk(f, f)); }
__device__ __forceinline__ f32x4 mfma(v4u a, v4u b, f32x4 c) { return __builtin_amdgcn_mfma_f32_16x16x32_bf16(__builtin_bit_cast(bf16x8, a), __builtin_bit_cast(bf16x8, b), c, 0, 0, 0); }
template <int CTRL> __device__ __forceinline__ float dpp(float x) { return asf(__builtin_amdgcn_mov_dpp((int)asu(x), CTRL, 0xf, 0xf, true)); }
constexpr int XOR1 = 0xB1, XOR2 = 0x4E, XOR7 = 0x141, ROR8 = 0x128;
__device__ __forceinline__ float vmax(float a, float b) { float r; asm("v_max_f32 %0, %1, %2" : "=v"(r) : "v"(a), "v"(b)); return r; }
__device__ __forceinline__ float xrow_max(float x) {
  auto s = __builtin_amdgcn_permlane16_swap(asu(x), asu(x), false, false); x = vmax(asf(s[0]), asf(s[1]));
  auto t = __builtin_amdgcn_permlane32_swap(asu(x), asu(x), false, false); return vmax(asf(t[0]), asf(t[1]));
}
__device__ __forceinline__ float xrow_sum(float x) {
  auto s = __builtin_amdgcn_permlane16_swap(asu(x), asu(x), false, false); x = asf(s[0]) + asf(s[1]);
  auto t = __builtin_amdgcn_permlane32_swap(asu(x), asu(x), false, false); return asf(t[0]) + asf(t[1]);
}
__device__ __forceinline__ float wave_sum(float x) { x += dpp<XOR1>(x); x += dpp<XOR2>(x); x += dpp<XOR7>(x); x += dpp<ROR8>(x); return xrow_sum(x); }
#define SB() __builtin_amdgcn_sched_barrier(0)
#define LGKM0() asm volatile("s_waitcnt lgkmcnt(0)" ::: "memory")
#define CBAR() asm volatile("" ::: "memory")
#define PINV(v) asm volatile("" : "+v"(v))
#define STR2(x) #x
#define STR(x) STR2(x)
constexpr f32x4 Z4 = {0.f, 0.f, 0.f, 0.f};

__device__ __forceinline__ void set_byte(int c, int i, unsigned v) {
  asm volatile("s_mov_b64 exec, 1\n\tds_write_b8 %0, %1\n\ts_mov_b64 exec, -1" :: "v"(L_CNT + c * 4 + i), "v"(v) : "memory");
}
__device__ __forceinline__ void set_word(int c, unsigned v) {
  asm volatile("s_mov_b64 exec, 1\n\tds_write_b32 %0, %1\n\ts_mov_b64 exec, -1" :: "v"(L_CNT + c * 4), "v"(v) : "memory");
}
__device__ __forceinline__ unsigned get_word(LAS char* L, int c) {
  const unsigned v = __builtin_amdgcn_readfirstlane(__hip_atomic_load((LAS unsigned*)(L + L_CNT) + c, __ATOMIC_RELAXED, __HIP_MEMORY_SCOPE_WORKGROUP));
  CBAR();
  return v;
}
template <int SLEEP> __device__ __forceinline__ void wait_eq(LAS char* L, int c, unsigned want, bool dry) {
#pragma unroll 1
  for (int i = 0; i < SPIN_L; ++i) {
    if (get_word(L, c) == want || dry) break;
    if (SLEEP) __builtin_amdgcn_s_sleep(SLEEP);
  }
}
template <int SLEEP> __device__ __forceinline__ void wait_prog(LAS char* L, unsigned& prog, int want, bool dry) {
  if (prog < (unsigned)want) {
#pragma unroll 1
    for (int i = 0; i < SPIN_L; ++i) {
      prog = get_word(L, C_PROG);
      if (prog >= (unsigned)want || dry) break;
      if (SLEEP) __builtin_amdgcn_s_sleep(SLEEP);
    }
  }
  CBAR();
}
__device__ __forceinline__ int unit_of(int k) { return (k >= NUNIT ? k - NUNIT : k) * 8192; }
__device__ __forceinline__ v4u trfrag(LAS char* a0, int d) {
  const s16x4 lo = __builtin_amdgcn_ds_read_tr16_b64_v4i16((LAS s16x4*)a0), hi = __builtin_amdgcn_ds_read_tr16_b64_v4i16((LAS s16x4*)(a0 + d));
  const v2u a = __builtin_bit_cast(v2u, lo), b = __builtin_bit_cast(v2u, hi);
  return v4u{a[0], a[1], b[0], b[1]};
}
__device__ __forceinline__ bool tagged(v4u v, unsigned tag) { return (v[1] == tag) & (v[3] == tag); }
__device__ __forceinline__ int poll_words(char* mb, int m, unsigned tag, int l) {
  GAS unsigned* p = (GAS unsigned*)mb + l;
  unsigned zero = 0; PINV(zero);
  int cnt = 0;
  if (l <= m) {
    unsigned a = __hip_atomic_fetch_add(p, zero, __ATOMIC_RELAXED, __HIP_MEMORY_SCOPE_AGENT), b;
    SB();
    __builtin_amdgcn_s_sleep(P_GAP);
#pragma unroll 1
    for (int it = 0; it < SPIN_G; ++it) {
      b = __hip_atomic_fetch_add(p, zero, __ATOMIC_RELAXED, __HIP_MEMORY_SCOPE_AGENT);
      SB();
      ++cnt;
      if (__all(a == tag)) break;
      a = __hip_atomic_fetch_add(p, zero, __ATOMIC_RELAXED, __HIP_MEMORY_SCOPE_AGENT);
      SB();
      ++cnt;
      if (__all(b == tag)) break;
    }
  }
  CBAR();
  return __builtin_amdgcn_readfirstlane(cnt);
}
__device__ __forceinline__ void issue4(rsrc_t rws, int off, int stride, v4u (&Q)[4]) {
#pragma unroll
  for (int t = 0; t < 4; ++t) Q[t] = ldg(rws, off, t * stride);
  SB();
}
__device__ __forceinline__ void check4(rsrc_t rws, int off, int stride, unsigned tag, v4u (&Q)[4]) {
#pragma unroll 1
  for (int it = 0; it < SPIN_G; ++it) {
    if (__all(tagged(Q[0], tag) & tagged(Q[1], tag) & tagged(Q[2], tag) & tagged(Q[3], tag))) break;
    CBAR();
    issue4(rws, off, stride, Q);
  }
}

#define ST(i) do { if (i == LEAK_I) { SB(); leak = __builtin_amdgcn_s_memrealtime(); SB(); } } while (0)
#define NP(v, e) (void)(e)

__global__ void __launch_bounds__(NTHR, 3) fused_decode(Params p) {
  __shared__ __attribute__((aligned(16))) char smem[L_BYTES];
  LAS char* L = (LAS char*)smem;
  const int tid = threadIdx.x, w = __builtin_amdgcn_readfirstlane(tid >> 6), l = tid & 63;
  const int c = blockIdx.x, x = c & 7, j = c >> 3;
  const int i16 = l & 15, g = l >> 4;
  unsigned long long leak = 0;
  ST(0);
  const int xp = x >> 1, xo = x & 1, t64 = xo * 32 + j;
  const int bb = 2 * xo + (j >> 4), sp = j & 15, pair = bb * 4 + xp;
  const int qks = c & 3, qcb = c >> 2;
  const int rb = 2 * x + (j >> 4), cb = sp;
  const bool big = xp < 2;
  const int rb0 = big ? 4 * xp : 8 + 3 * (xp - 2);
  const bool has_gu = big || j >= 8, has_dn = big || j < 16 || ((j >> 3) & 1) == xo;
  const int gt = big ? t64 : xo * 24 + j - 8, gsrc = gt & 3, gcb = rb0 * 4 + (gt >> 2);
  const int rbd = rb0 + (big ? (t64 >> 4) : (j < 16 ? xo : 2));
  const int k_wo = has_gu ? 20 : 12, k_dn = k_wo + 4;

  if (__builtin_expect(w == 9, 1)) {
    __builtin_amdgcn_s_setprio(3);
    int k = 0, unit = 0;
#define TILE(WN, r, v0, v1, v2, v3, sbase, istride) do { \
      constexpr int DD = (WN - 2) / 8, N0 = (WN - 2) % 8; \
      const rsrc_t r_ = (r); const int sb_ = (sbase), is_ = (istride); \
      _Pragma("unroll 1") for (int t = 0; t < 4; ++t) { \
        if (k >= NUNIT) { \
          if (k < NUNIT + 4) { _Pragma("unroll 1") for (int i = 0; i < SPIN_L; ++i) { const unsigned v = get_word(L, C_PJ); if ((v & 0x03030303u) && (v & 0xff) && (v & 0xff00) && (v & 0xff0000) && (v >> 24)) break; } } \
          else if (k < NUNIT + 8) wait_eq<1>(L, C_KD, ALL1, false); \
          else { wait_eq<1>(L, C_G2A, ALL1, false); wait_eq<1>(L, C_G2B, ALL1, false); } \
        } \
        const int so = sb_ + t * 8 * is_; \
        const unsigned m = (unsigned)(uintptr_t)L + unit * 8192; \
        _Pragma("unroll") for (int n = 0; n < 8; ++n) { \
          asm volatile("s_mov_b32 m0, %0\n\ts_nop 0\n\tbuffer_load_dwordx4 %1, %2, %3 offen" NT_STR " lds\n\ts_waitcnt vmcnt(" STR(WN - 1) ")" \
                       :: "s"(m + n * 1024), "v"((n & 3) == 0 ? (v0) : (n & 3) == 1 ? (v1) : (n & 3) == 2 ? (v2) : (v3)), "s"(r_), "s"(so + n * is_) : "memory"); \
          if (n == N0 && k > DD) set_word(C_PROG, (unsigned)(k - DD)); \
        } \
        if (k == 0) __builtin_amdgcn_s_barrier(); \
        ++k; if (++unit == NUNIT) unit = 0; \
      } } while (0)
    const int r8 = l >> 3, ce = ((l & 7) ^ (r8 & 2)) * 16, co = ce ^ 64;
    const int kvo = (pair * 8192 + 6144 + sp * 128) * 256;
    const int kb = g * 256 + ((i16 ^ g) * 16), vb = g * 256 + ((i16 ^ (2 * g)) * 16);
#pragma unroll 1
    for (int tl = 0; tl < 7; ++tl) {
      if ((tl == 3 || tl == 4) && !has_gu) continue;
      if (tl == 6 && !has_dn) continue;
      const u16* base = tl == 0 ? p.wq : tl == 1 ? p.kc : tl == 2 ? p.vc : tl < 5 ? ((gsrc & 2) ? p.wu : p.wg) : tl == 5 ? p.wo : p.wd;
      const int bytes = tl == 0 || tl == 5 ? 8 << 20 : tl < 3 ? 32 << 20 : 7 << 20;
      const int ld = tl == 0 ? 8192 : tl < 3 ? 128 : tl < 5 ? 7168 : 2048;
      const int sb = tl == 0 ? qks * 256 * 8192 + qcb * 128 : tl < 3 ? kvo : tl < 5 ? ((gsrc & 1) * 512 + (tl - 3) * 256) * 7168 + gcb * 128
                   : (tl == 5 ? rb : rbd) * 256 * 2048 + cb * 128;
      const bool kv = tl == 1 || tl == 2;
      const int v0 = tl == 1 ? kb : tl == 2 ? vb : r8 * ld + ce;
      const int x1 = tl == 1 ? 64 : tl == 2 ? 128 : 64, x2 = tl == 1 ? 128 : 0;
      (void)kv; (void)co;
      if (WIN1 == WIN2 || tl < 5) TILE(WIN1, mk(base, bytes), v0, v0 ^ x1, v0 ^ x2, v0 ^ x1 ^ x2, sb, 8 * ld);
      else TILE(WIN2, mk(base, bytes), v0, v0 ^ x1, v0 ^ x2, v0 ^ x1 ^ x2, sb, 8 * ld);
      if (tl + 1 == LEAK_I) { SB(); leak = __builtin_amdgcn_s_memrealtime(); SB(); }
    }
    if ((WIN2 - 2) / 8 >= 3) { asm volatile("s_waitcnt vmcnt(24)" ::: "memory"); set_word(C_PROG, (unsigned)(k - 3)); }
    if ((WIN2 - 2) / 8 >= 2) { asm volatile("s_waitcnt vmcnt(16)" ::: "memory"); set_word(C_PROG, (unsigned)(k - 2)); }
    asm volatile("s_waitcnt vmcnt(8)" ::: "memory"); set_word(C_PROG, (unsigned)(k - 1));
    asm volatile("s_waitcnt vmcnt(0)" ::: "memory"); set_word(C_PROG, (unsigned)k);
    ST(8);
  } else if (w < 8) {
    const rsrc_t rws = mk(p.ws, WS_END);
    char* mb = p.ws + WS_MB + c * 1024;
    const int w4 = w & 3, q4 = i16 >> 2, pp = l & 3;
    const int wa = (8 * g + q4) * 128 + (((2 * w4 + (pp >> 1)) ^ (2 * (((q4 >> 1) & 1) | ((g & 1) << 1)))) * 16) + 8 * (pp & 1);
    unsigned tag = 0, prog = 0;
    if (w >= 4) {
      const rsrc_t r = mk(p.x, 4 * 1024 * 2);
      const v4u a = __builtin_amdgcn_raw_buffer_load_b128(r, w4 * 2048 + l * 16, 0, 0), b = __builtin_amdgcn_raw_buffer_load_b128(r, w4 * 2048 + 1024 + l * 16, 0, 0);
      SB();
      __builtin_amdgcn_s_barrier();
      CBAR();
      SB();
      float v[16];
#pragma unroll
      for (int n = 0; n < 4; ++n) { v[2 * n] = bflo(a[n]); v[2 * n + 1] = bfhi(a[n]); v[8 + 2 * n] = bflo(b[n]); v[9 + 2 * n] = bfhi(b[n]); }
      float s = 0.f;
#pragma unroll
      for (int n = 0; n < 16; ++n) s += v[n];
      const float mean = wave_sum(s) * (1.f / 1024.f);
      float q = 0.f;
#pragma unroll
      for (int n = 0; n < 16; ++n) { const float d = v[n] - mean; q = fmaf(d, d, q); }
      const float var = rbf(wave_sum(q) * (1.f / 1024.f));
      const float mb_ = rbf(mean);
      const float rs = __builtin_amdgcn_rcpf(rbf(__builtin_amdgcn_sqrtf(rbf(var + 1e-5f))));
      v4u oa, ob;
#pragma unroll
      for (int n = 0; n < 4; ++n) {
        const unsigned da = pk(v[2 * n] - mb_, v[2 * n + 1] - mb_), db = pk(v[8 + 2 * n] - mb_, v[9 + 2 * n] - mb_);
        oa[n] = pk(bflo(da) * rs, bfhi(da) * rs); ob[n] = pk(bflo(db) * rs, bfhi(db) * rs);
      }
      *(LAS v4u*)(L + L_XN + w4 * 2048 + l * 16) = oa;
      *(LAS v4u*)(L + L_XN + w4 * 2048 + 1024 + l * 16) = ob;
      set_byte(C_XN, w4, 1u);
      ST(1);
    } else {
      __builtin_amdgcn_s_barrier();
      CBAR();
    }
#pragma unroll 1
    for (int pass = 0; pass < 2; ++pass) {
      int dryi = DRY ? 1 - pass : 0; asm volatile("" : "+s"(dryi));
      const bool dry = dryi != 0;
      if (!DRY && pass == 0 && w < 4) continue;
      const unsigned one = dry ? 0u : 1u;
      if (w < 4) {
        wait_eq<2>(L, C_TAGV, 1u, dry);
        tag = get_word(L, C_TAG);
        const int qoff = WS_GQ + (bb * 2048 + xp * 512 + w * 128 + l * 2) * 8;
        const int h = 2 * w + (l >> 5);
        int qw = L_QS + h * 256 + ((((l & 31) >> 1) ^ (2 * h)) * 16) + (l & 1) * 8;
        int qr = L_QS + (i16 & 7) * 256 + ((g ^ (2 * (i16 & 7))) * 16);
        int ka = (4 + w) * 8192 + i16 * 256 + ((g ^ i16) * 16);
        PINV(qw); PINV(qr); PINV(ka);
        wait_prog<2>(L, prog, 4, dry);
        ST(1);
        v4u Q[4] = {};
        if (!dry) { NP(np0, poll_words(mb + MB_Q + w * 64, 15, tag, l)); ST(2); issue4(rws, qoff, 4 * 2048 * 8, Q); }
        wait_prog<1>(L, prog, 5 + w, dry);
        v4u KF[8];
#pragma unroll
        for (int n = 0; n < 8; ++n) KF[n] = *(LAS v4u*)(L + ((ka ^ ((n & 3) * 64)) + (n >> 2) * 4096));
        set_byte(C_KD, w, one);
        SB();
        if (!dry) check4(rws, qoff, 4 * 2048 * 8, tag, Q);
        ST(3);
        const float a0 = (bflo(Q[0][0]) + bflo(Q[1][0])) + (bflo(Q[2][0]) + bflo(Q[3][0]));
        const float a1 = (bfhi(Q[0][0]) + bfhi(Q[1][0])) + (bfhi(Q[2][0]) + bfhi(Q[3][0]));
        const float a2 = (bflo(Q[0][2]) + bflo(Q[1][2])) + (bflo(Q[2][2]) + bflo(Q[3][2]));
        const float a3 = (bfhi(Q[0][2]) + bfhi(Q[1][2])) + (bfhi(Q[2][2]) + bfhi(Q[3][2]));
        *(LAS v2u*)(L + qw) = v2u{pk(a0, a1), pk(a2, a3)};
        set_byte(C_QS, w, one);
        wait_eq<0>(L, C_QS, ALL1, dry);
        v4u qf[4];
#pragma unroll
        for (int s = 0; s < 4; ++s) qf[s] = *(LAS v4u*)(L + (qr ^ (s * 64)));
        f32x4 d0 = Z4, d1 = Z4;
#pragma unroll
        for (int s = 0; s < 4; ++s) { d0 = mfma(KF[s], qf[s], d0); d1 = mfma(KF[4 + s], qf[s], d1); }
        float sc[8] = {d0[0], d0[1], d0[2], d0[3], d1[0], d1[1], d1[2], d1[3]};
#pragma unroll
        for (int n = 0; n < 8; n += 2) {
          const unsigned a = pk(sc[n], sc[n + 1]);
          const unsigned b = pk(bflo(a) * 0.08838834764831845f, bfhi(a) * 0.08838834764831845f);
          sc[n] = bflo(b); sc[n + 1] = bfhi(b);
        }
        float m = __builtin_fmaxf(__builtin_fmaxf(__builtin_fmaxf(sc[0], sc[1]), sc[2]), __builtin_fmaxf(__builtin_fmaxf(sc[3], sc[4]), sc[5]));
        m = __builtin_fmaxf(__builtin_fmaxf(m, sc[6]), sc[7]);
        m = xrow_max(m) * 1.4426950408889634f;
#pragma unroll
        for (int n = 0; n < 8; ++n) sc[n] = __builtin_amdgcn_exp2f(fmaf(sc[n], 1.4426950408889634f, -m));
        *(LAS v4u*)(L + L_PS + w * 1024 + l * 16) = v4u{pk(sc[0], sc[1]), pk(sc[2], sc[3]), pk(sc[4], sc[5]), pk(sc[6], sc[7])};
        const float ls = xrow_sum(((sc[0] + sc[1]) + (sc[2] + sc[3])) + ((sc[4] + sc[5]) + (sc[6] + sc[7])));
        *(LAS f2_t*)(L + L_ML + i16 * 32 + w * 8) = f2_t{m, ls};
        set_byte(C_P, w, one);
        ST(4);
      }
      if (w < 4 || pass == 1 || true) {
      }
      if (w >= 4 && pass == 0) {
      }
      if (w < 4 || pass == 1) {
        const int rr = 4 * g + q4;
        int va = 8 * 8192 + rr * 256 + (((2 * w + (pp >> 1)) ^ (2 * (rr & 7))) * 16) + 8 * (pp & 1);
        int goff = i16 < 8 && !dry ? WS_G2 + ((pair * 16 + 2 * i16 + (w >> 2)) * 16 + sp) * 384 + w4 * 64 + g * 16 : OOB;
        int loff = l < 8 && w4 == 0 && !dry ? goff + 256 : OOB;
        int noff = l < 8 && !dry ? WS_MB + (((j & 16) + 2 * l + (w >> 2)) * 8 + x) * 1024 + MB_2 + (sp * 4 + w4) * 4 : OOB;
        PINV(va); PINV(goff); PINV(loff); PINV(noff);
        v4u VF[4];
#pragma unroll
        for (int t = 0; t < 3; ++t) { wait_prog<1>(L, prog, 9 + t, dry); VF[t] = trfrag(L + va + t * 8192, 4096); }
        wait_eq<0>(L, C_P, ALL1, dry);
        v4u PF[4];
#pragma unroll
        for (int t = 0; t < 4; ++t) PF[t] = *(LAS v4u*)(L + L_PS + t * 1024 + l * 16);
        const f32x4 ml0 = *(LAS f32x4*)(L + L_ML + i16 * 32), ml1 = *(LAS f32x4*)(L + L_ML + i16 * 32 + 16);
        const f32x4 A0 = mfma(VF[0], PF[0], Z4), A1 = mfma(VF[1], PF[1], Z4), A2 = mfma(VF[2], PF[2], Z4);
        const float M = vmax(vmax(ml0[0], ml0[2]), vmax(ml1[0], ml1[2]));
        float a0 = __builtin_amdgcn_exp2f(ml0[0] - M), a1 = __builtin_amdgcn_exp2f(ml0[2] - M), a2 = __builtin_amdgcn_exp2f(ml1[0] - M), a3 = __builtin_amdgcn_exp2f(ml1[2] - M);
        const float Ls = (a0 * ml0[1] + a1 * ml0[3]) + (a2 * ml1[1] + a3 * ml1[3]);
        const float inv = __builtin_amdgcn_rcpf(Ls);
        float lse = M + __builtin_amdgcn_logf(Ls);
        f32x4 ov = (A0 * a0 + A1 * a1 + A2 * a2) * inv;
        a3 *= inv;
        PINV(ov); PINV(a3); PINV(lse);
        ST(5);
        wait_prog<0>(L, prog, 12, dry);
        ov += mfma(trfrag(L + va + 3 * 8192, 4096), PF[3], Z4) * a3;
        stg(rws, goff, v4u{pk(ov[0], ov[1]), tag, pk(ov[2], ov[3]), tag});
        stg2(rws, loff, v2u{asu(lse), tag});
        stg1(rws, noff, tag);
        set_byte(C_G2A + (w >> 2), w4, one);
        ST(6);
      }
      if (w >= 4) {
        const bool job = pass == 1;
        if (!job) { wait_eq<1>(L, C_XN, ALL1, false); wait_eq<1>(L, C_TAGV, 1u, false); tag = get_word(L, C_TAG); }
        if (!job || has_gu) {
          const int k0 = job ? 12 : 0, nt = job ? 8 : 4;
          LAS char* xa = L + L_XN + pp * 2048 + ((job ? (gsrc & 1) * 512 : qks * 256) + g * 8) * 2;
          int soff = i16 >= 4 ? OOB : job ? WS_GGU + ((gsrc * 4 + pp) * 1792 + gcb * 32 + w4 * 8 + g * 2) * 8 : WS_GQ + ((qks * 4 + pp) * 2048 + qcb * 32 + w4 * 8 + g * 2) * 8;
          const int rl = gt >> 4;
          const int gj = big ? (rl & 1) * 16 + l : (rl < 2 ? l : 16 + l), gx = big ? rl >> 1 : (rl < 2 ? rl : (l >> 3) & 1);
          int noff = job ? (l < 16 ? WS_MB + (gj * 8 + 2 * xp + gx) * 1024 + MB_G + (gt & 15) * 4 : OOB)
                         : WS_MB + ((l & 31) * 8 + 2 * (qcb >> 4) + (l >> 5)) * 1024 + MB_Q + ((qcb & 15) * 4 + qks) * 4;
          PINV(soff); PINV(noff);
          f32x4 acc0 = Z4, acc1 = Z4;
#pragma unroll 1
          for (int t = 0; t < nt; ++t) {
            const v4u x0 = *(LAS v4u*)(xa + t * 128), x1 = *(LAS v4u*)(xa + t * 128 + 64);
            LAS char* u = L + unit_of(k0 + t) + wa;
            wait_prog<1>(L, prog, k0 + t + 1, false);
            acc0 = mfma(trfrag(u, 512), x0, acc0);
            acc1 = mfma(trfrag(u + 4096, 512), x1, acc1);
          }
          if (job) ST(7); else ST(2);
          acc0 += acc1;
          stg(rws, soff, v4u{pk(acc0[0], acc0[1]), tag, pk(acc0[2], acc0[3]), tag});
          set_byte(C_PJ, w4, job ? 2u : 1u);
          if (w4 == 0) { wait_eq<0>(L, C_PJ, job ? ALL2 : ALL1, false); stg1(rws, noff, tag); }
          if (job) ST(8); else ST(3);
        } else wait_prog<4>(L, prog, k_dn + 4, false);
        if (has_dn) {
          const int hoff = WS_GGU + (g * 1792 + rbd * 128 + w4 * 32 + i16 * 2) * 8;
          v4u H[4] = {};
          if (job) {
            NP(np0, poll_words(mb + MB_G + w4 * 16, 3, tag, l));
            ST(9);
            issue4(rws, hoff, 4 * 1792 * 8, H);
            check4(rws, hoff, 4 * 1792 * 8, tag, H);
            ST(10);
          }
          const unsigned g0 = pk(bflo(H[0][0]) + bflo(H[1][0]), bfhi(H[0][0]) + bfhi(H[1][0])), g1 = pk(bflo(H[0][2]) + bflo(H[1][2]), bfhi(H[0][2]) + bfhi(H[1][2]));
          const unsigned u0 = pk(bflo(H[2][0]) + bflo(H[3][0]), bfhi(H[2][0]) + bfhi(H[3][0])), u1 = pk(bflo(H[2][2]) + bflo(H[3][2]), bfhi(H[2][2]) + bfhi(H[3][2]));
          *(LAS v2u*)(L + L_HF + g * 528 + w4 * 128 + i16 * 8) = v2u{pk(bflo(g0) * bflo(u0), bfhi(g0) * bfhi(u0)), pk(bflo(g1) * bflo(u1), bfhi(g1) * bfhi(u1))};
          set_byte(C_HF, w4, job ? 1u : 0u);
        }
      } else {
        int aa = pp * 528 + g * 16;
        const bool u1 = i16 & 4;
        int soff = i16 < 8 && !dry ? WS_G5 + ((cb * 16 + pp * 4 + w) * 16 + rb) * 128 + g * 32 + (u1 ? 16 : 0) : OOB;
        int noff = l < 4 && !dry ? WS_MB + (((w & 1) * 16 + cb) * 8 + 2 * l + (w >> 1)) * 1024 + MB_5 + rb * 4 : OOB;
        PINV(aa); PINV(soff); PINV(noff);
        v4u WF[8], DF[8];
#pragma unroll
        for (int t = 0; t < 4; ++t) {
          LAS char* u = L + unit_of(k_wo + t) + wa;
          wait_prog<1>(L, prog, k_wo + t + 1, dry);
          WF[2 * t] = trfrag(u, 512); WF[2 * t + 1] = trfrag(u + 4096, 512);
        }
        if (has_dn) {
#pragma unroll
          for (int t = 0; t < 3; ++t) {
            LAS char* u = L + unit_of(k_dn + t) + wa;
            wait_prog<1>(L, prog, k_dn + t + 1, dry);
            DF[2 * t] = trfrag(u, 512); DF[2 * t + 1] = trfrag(u + 4096, 512);
          }
        }
        ST(7);
        f32x4 acc0 = Z4, acc1 = Z4;
        wait_eq<0>(L, C_OF, 1u, dry);
#pragma unroll
        for (int s = 0; s < 8; s += 2) { acc0 = mfma(WF[s], *(LAS v4u*)(L + L_OF + aa + s * 64), acc0); acc1 = mfma(WF[s + 1], *(LAS v4u*)(L + L_OF + aa + s * 64 + 64), acc1); }
        ST(8);
        if (has_dn) {
          LAS char* u = L + unit_of(k_dn + 3) + wa;
          wait_eq<0>(L, C_HF, ALL1, dry);
          v4u hf[8];
#pragma unroll
          for (int s = 0; s < 8; ++s) hf[s] = *(LAS v4u*)(L + L_HF + aa + s * 64);
#pragma unroll
          for (int s = 0; s < 6; s += 2) { acc0 = mfma(DF[s], hf[s], acc0); acc1 = mfma(DF[s + 1], hf[s + 1], acc1); }
          ST(9);
          wait_prog<0>(L, prog, k_dn + 4, dry);
          acc0 = mfma(trfrag(u, 512), hf[6], acc0);
          acc1 = mfma(trfrag(u + 4096, 512), hf[7], acc1);
        }
        acc0 += acc1;
        stg(rws, soff, v4u{asu(u1 ? acc0[2] : acc0[0]), tag, asu(u1 ? acc0[3] : acc0[1]), tag});
        stg1(rws, noff, tag);
        set_byte(C_G5, w, one);
        ST(10);
      }
    }
  } else {
    __builtin_amdgcn_s_setprio(2);
    const rsrc_t rws = mk(p.ws, WS_END);
    char* mb = p.ws + WS_MB + c * 1024;
    const unsigned ev = __builtin_amdgcn_raw_buffer_load_b32(rws, WS_E, 0, AUX_SC1);
    const unsigned warm = __builtin_amdgcn_raw_buffer_load_b32(rws, WS_END - 4, 0, AUX_SC1);
    const int oidx = (rb >> 2) * 1024 + cb * 64 + (rb & 3) * 16 + (l & 7) * 2;
    const unsigned xres = *(const unsigned*)(p.x + oidx);
    if (l < 16) *((LAS unsigned*)(L + L_CNT) + l) = 0u;
    LGKM0();
    SB();
    __builtin_amdgcn_s_barrier();
    CBAR();
    SB();
    const unsigned tag = __builtin_amdgcn_readfirstlane(ev) + 1u;
    set_word(C_TAG, tag);
    set_word(C_TAGV, 1u);
    asm volatile("" :: "v"(warm));
    const rsrc_t rout = mk(p.out, 8192);
#pragma unroll 1
    for (int pass = DRY ? 0 : 1; pass < 2; ++pass) {
      int dryi = 1 - pass; asm volatile("" : "+s"(dryi));
      const bool dry = dryi != 0;
      const int off2 = WS_G2 + ((pair * 16 + sp) * 16 + g) * 384;
      int soff = l < 16 && !dry ? WS_GO + (bb * 2048 + xp * 512 + sp * 32 + l * 2) * 8 : OOB;
      int noff = l < 16 && !dry ? WS_MB + ((((sp >> 2) & 1) * 16 + l) * 8 + 2 * xp + (sp >> 3)) * 1024 + MB_O + (bb * 4 + (sp & 3)) * 4 : OOB;
      int ooff = l < 8 && !dry ? oidx * 2 : OOB;
      PINV(soff); PINV(noff); PINV(ooff);
      wait_eq<2>(L, C_G2A, ALL1, dry);
      ST(1);
      v4u P[4] = {}; v2u S[4] = {};
      if (!dry) {
        NP(np0, poll_words(mb + MB_2, 63, tag, l));
        ST(2);
#pragma unroll 1
        for (int it = 0; it < SPIN_G; ++it) {
          CBAR();
          bool ok = true;
#pragma unroll
          for (int t = 0; t < 4; ++t) { P[t] = ldg(rws, off2 + i16 * 16, t * 4 * 384); S[t] = ldg2(rws, off2 + 256, t * 4 * 384); }
#pragma unroll
          for (int t = 0; t < 4; ++t) ok &= tagged(P[t], tag) & (S[t][1] == tag);
          if (__all(ok)) break;
        }
      }
      ST(3);
      const float M = xrow_max(vmax(vmax(asf(S[0][0]), asf(S[1][0])), vmax(asf(S[2][0]), asf(S[3][0]))));
      float ws = 0.f, o0 = 0.f, o1 = 0.f, o2 = 0.f, o3 = 0.f;
#pragma unroll
      for (int t = 0; t < 4; ++t) {
        const float a = __builtin_amdgcn_exp2f(asf(S[t][0]) - M);
        ws += a; o0 = fmaf(a, bflo(P[t][0]), o0); o1 = fmaf(a, bfhi(P[t][0]), o1); o2 = fmaf(a, bflo(P[t][2]), o2); o3 = fmaf(a, bfhi(P[t][2]), o3);
      }
      const float inv = __builtin_amdgcn_rcpf(xrow_sum(ws));
      o0 = xrow_sum(o0) * inv; o1 = xrow_sum(o1) * inv; o2 = xrow_sum(o2) * inv; o3 = xrow_sum(o3) * inv;
      stg(rws, soff, v4u{pk(o0, o1), tag, pk(o2, o3), tag});
      stg1(rws, noff, tag);
      ST(4);
      v4u O[4] = {};
      if (!dry) {
        NP(np1, poll_words(mb + MB_O, 15, tag, l));
        ST(5);
        issue4(rws, WS_GO + (rb * 128 + l * 2) * 8, 2048 * 8, O);
        check4(rws, WS_GO + (rb * 128 + l * 2) * 8, 2048 * 8, tag, O);
      }
#pragma unroll
      for (int t = 0; t < 4; ++t) *(LAS v2u*)(L + L_OF + t * 528 + l * 8) = v2u{O[t][0], O[t][2]};
      set_word(C_OF, dry ? 0u : 1u);
      ST(6);
      wait_eq<1>(L, C_G5, ALL1, dry);
      ST(7);
      v4u P0 = {}, P1 = {};
      if (!dry) {
        NP(np2, poll_words(mb + MB_5, 15, tag, l));
        ST(8);
        const int off = WS_G5 + ((cb * 16 + rb) * 256 + l * 2) * 8;
#pragma unroll 1
        for (int it = 0; it < SPIN_G; ++it) {
          CBAR();
          P0 = ldg(rws, off, 0); P1 = ldg(rws, off, 1024);
          if (__all(tagged(P0, tag) & tagged(P1, tag))) break;
        }
      }
      ST(9);
      float a = asf(P0[0]) + asf(P1[0]), b = asf(P0[2]) + asf(P1[2]);
      a += dpp<ROR8>(a); b += dpp<ROR8>(b);
      a = xrow_sum(a); b = xrow_sum(b);
      __builtin_amdgcn_raw_buffer_store_b32(pk(bflo(xres) + a, bfhi(xres) + b), rout, ooff, 0, 0);
      stg1(rws, c == 0 && l == 0 && !dry ? WS_E : OOB, tag);
      ST(10);
    }
  }
  if (w >= LEAK_W0 && w <= LEAK_W1 && leak && (LEAK_X < 0 || x == LEAK_X)) { while (__builtin_amdgcn_s_memrealtime() - leak < 3000ull) __builtin_amdgcn_s_sleep(2); }
}

extern "C" void kernel_launch(void* const* d_in, const int* in_sizes, int n_in, void* d_out, int out_size, void* d_ws, size_t ws_size, hipStream_t stream) {
  Params p;
  p.x = (const u16*)d_in[0]; p.kc = (const u16*)d_in[1]; p.vc = (const u16*)d_in[2]; p.wq = (const u16*)d_in[3]; p.wo = (const u16*)d_in[4];
  p.wg = (const u16*)d_in[6]; p.wu = (const u16*)d_in[7]; p.wd = (const u16*)d_in[8]; p.out = (u16*)d_out; p.ws = (char*)d_ws;
  fused_decode<<<256, NTHR, 0, stream>>>(p);
}
```

```cpp
#define DRY 0
#include <hip/hip_runtime.h>
#include <hip/hip_bf16.h>
#include <stdint.h>

#define LEAK_X -1
#define STAMPS 0
#define WIN1 26
#define WIN2 26
#define NT_STR " nt"
#define P_GAP 5

typedef unsigned v4u __attribute__((ext_vector_type(4)));
typedef unsigned v2u __attribute__((ext_vector_type(2)));
typedef float f32x4 __attribute__((ext_vector_type(4)));
typedef short bf16x8 __attribute__((ext_vector_type(8)));
typedef short s16x4 __attribute__((ext_vector_type(4)));
typedef unsigned short u16;
#define LAS __attribute__((address_space(3)))
#define GAS __attribute__((address_space(1)))
typedef __amdgpu_buffer_rsrc_t rsrc_t;

constexpr int NTHR = 640;
constexpr int WS_E = 0;
constexpr int WS_MB = 4096, MB_Q = 0, MB_2 = 256, MB_O = 512, MB_G = 640, MB_5 = 768;
constexpr int WS_GQ = WS_MB + 256 * 1024;
constexpr int WS_GO = WS_GQ + 4 * 4 * 2048 * 8;
constexpr int WS_GGU = WS_GO + 4 * 2048 * 8;
constexpr int WS_G5 = WS_GGU + 4 * 4 * 1792 * 8;
constexpr int WS_G2 = WS_G5 + 16 * 16 * 16 * 16 * 8;
constexpr int WS_END = WS_G2 + 16 * 16 * 16 * 384;
constexpr int WS_STAMP = 8 << 20;
constexpr int NUNIT = 17;
constexpr int L_XN = NUNIT * 8192;
constexpr int L_QS = L_XN + 8192;
constexpr int L_PS = L_QS + 2048;
constexpr int L_OF = L_PS + 4096;
constexpr int L_HF = L_OF + 2112;
constexpr int L_ML = L_HF + 2112;
constexpr int L_CNT = L_ML + 512;
constexpr int L_BYTES = L_CNT + 64;
static_assert(L_BYTES <= 160 * 1024, "LDS");
enum { C_PROG = 0, C_XN = 1, C_TAGV = 2, C_TAG = 3, C_QS = 4, C_P = 5, C_PJ = 6, C_HF = 7, C_OF = 8, C_G5 = 9, C_G2A = 10, C_G2B = 11, C_KD = 12 };
constexpr unsigned ALL1 = 0x01010101u, ALL2 = 0x02020202u;
constexpr int SPIN_G = 1 << 14, SPIN_L = 1 << 20;
constexpr int AUX_SC1 = 16;

struct Params { const u16 *x, *kc, *vc, *wq, *wo, *wg, *wu, *wd; u16* out; char* ws; };

__device__ __forceinline__ rsrc_t mk(const void* p, int bytes) { return __builtin_amdgcn_make_buffer_rsrc((void*)p, 0, bytes, 0x00020000); }
__device__ __forceinline__ v4u ldg(rsrc_t r, int voff, int soff) { return __builtin_amdgcn_raw_buffer_load_b128(r, voff, soff, AUX_SC1); }
__device__ __forceinline__ v2u ldg2(rsrc_t r, int voff, int soff) { return __builtin_amdgcn_raw_buffer_load_b64(r, voff, soff, AUX_SC1); }
__device__ __forceinline__ void stg(rsrc_t r, int voff, v4u v) { __builtin_amdgcn_raw_buffer_store_b128(v, r, voff, 0, AUX_SC1); }
__device__ __forceinline__ void stg2(rsrc_t r, int voff, v2u v) { __builtin_amdgcn_raw_buffer_store_b64(v, r, voff, 0, AUX_SC1); }
__device__ __forceinline__ void stg1(rsrc_t r, int voff, unsigned v) { __builtin_amdgcn_raw_buffer_store_b32(v, r, voff, 0, AUX_SC1); }
constexpr int OOB = 0x7ffffff0;
__device__ __forceinline__ float asf(unsigned u) { return __builtin_bit_cast(float, u); }
__device__ __forceinline__ unsigned asu(float f) { return __builtin_bit_cast(unsigned, f); }
__device__ __forceinline__ float bflo(unsigned w) { return asf(w << 16); }
__device__ __forceinline__ float bfhi(unsigned w) { return asf(w & 0xffff0000u); }
typedef __bf16 bf2_t __attribute__((ext_vector_type(2)));
typedef float f2_t __attribute__((ext_vector_type(2)));
__device__ __forceinline__ unsigned pk(float lo, float hi) { f2_t f = {lo, hi}; return __builtin_bit_cast(unsigned, __builtin_convertvector(f, bf2_t)); }
__device__ __forceinline__ float rbf(float f) { return bflo(pk(f, f)); }
__device__ __forceinline__ f32x4 mfma(v4u a, v4u b, f32x4 c) { return __builtin_amdgcn_mfma_f32_16x16x32_bf16(__builtin_bit_cast(bf16x8, a), __builtin_bit_cast(bf16x8, b), c, 0, 0, 0); }
template <int CTRL> __device__ __forceinline__ float dpp(float x) { return asf(__builtin_amdgcn_mov_dpp((int)asu(x), CTRL, 0xf, 0xf, true)); }
constexpr int XOR1 = 0xB1, XOR2 = 0x4E, XOR7 = 0x141, ROR8 = 0x128;
__device__ __forceinline__ float vmax(float a, float b) { float r; asm("v_max_f32 %0, %1, %2" : "=v"(r) : "v"(a), "v"(b)); return r; }
__device__ __forceinline__ float xrow_max(float x) {
  auto s = __builtin_amdgcn_permlane16_swap(asu(x), asu(x), false, false); x = vmax(asf(s[0]), asf(s[1]));
  auto t = __builtin_amdgcn_permlane32_swap(asu(x), asu(x), false, false); return vmax(asf(t[0]), asf(t[1]));
}
__device__ __forceinline__ float xrow_sum(float x) {
  auto s = __builtin_amdgcn_permlane16_swap(asu(x), asu(x), false, false); x = asf(s[0]) + asf(s[1]);
  auto t = __builtin_amdgcn_permlane32_swap(asu(x), asu(x), false, false); return asf(t[0]) + asf(t[1]);
}
__device__ __forceinline__ float wave_sum(float x) { x += dpp<XOR1>(x); x += dpp<XOR2>(x); x += dpp<XOR7>(x); x += dpp<ROR8>(x); return xrow_sum(x); }
#define SB() __builtin_amdgcn_sched_barrier(0)
#define LGKM0() asm volatile("s_waitcnt lgkmcnt(0)" ::: "memory")
#define CBAR() asm volatile("" ::: "memory")
#define PINV(v) asm volatile("" : "+v"(v))
#define STR2(x) #x
#define STR(x) STR2(x)
constexpr f32x4 Z4 = {0.f, 0.f, 0.f, 0.f};

__device__ __forceinline__ void set_byte(int c, int i, unsigned v) {
  asm volatile("s_mov_b64 exec, 1\n\tds_write_b8 %0, %1\n\ts_mov_b64 exec, -1" :: "v"(L_CNT + c * 4 + i), "v"(v) : "memory");
}
__device__ __forceinline__ void set_word(int c, unsigned v) {
  asm volatile("s_mov_b64 exec, 1\n\tds_write_b32 %0, %1\n\ts_mov_b64 exec, -1" :: "v"(L_CNT + c * 4), "v"(v) : "memory");
}
__device__ __forceinline__ unsigned get_word(LAS char* L, int c) {
  const unsigned v = __builtin_amdgcn_readfirstlane(__hip_atomic_load((LAS unsigned*)(L + L_CNT) + c, __ATOMIC_RELAXED, __HIP_MEMORY_SCOPE_WORKGROUP));
  CBAR();
  return v;
}
template <int SLEEP> __device__ __forceinline__ void wait_eq(LAS char* L, int c, unsigned want, bool dry) {
#pragma unroll 1
  for (int i = 0; i < SPIN_L; ++i) {
    if (get_word(L, c) == want || dry) break;
    if (SLEEP) __builtin_amdgcn_s_sleep(SLEEP);
  }
}
template <int SLEEP> __device__ __forceinline__ void wait_prog(LAS char* L, unsigned& prog, int want, bool dry) {
  if (prog < (unsigned)want) {
#pragma unroll 1
    for (int i = 0; i < SPIN_L; ++i) {
      prog = get_word(L, C_PROG);
      if (prog >= (unsigned)want || dry) break;
      if (SLEEP) __builtin_amdgcn_s_sleep(SLEEP);
    }
  }
  CBAR();
}
__device__ __forceinline__ int unit_of(int k) { return (k >= NUNIT ? k - NUNIT : k) * 8192; }
__device__ __forceinline__ v4u trfrag(LAS char* a0, int d) {
  const s16x4 lo = __builtin_amdgcn_ds_read_tr16_b64_v4i16((LAS s16x4*)a0), hi = __builtin_amdgcn_ds_read_tr16_b64_v4i16((LAS s16x4*)(a0 + d));
  const v2u a = __builtin_bit_cast(v2u, lo), b = __builtin_bit_cast(v2u, hi);
  return v4u{a[0], a[1], b[0], b[1]};
}
__device__ __forceinline__ bool tagged(v4u v, unsigned tag) { return (v[1] == tag) & (v[3] == tag); }
__device__ __forceinline__ int poll_words(char* mb, int m, unsigned tag, int l) {
  GAS unsigned* p = (GAS unsigned*)mb + l;
  unsigned zero = 0; PINV(zero);
  int cnt = 0;
  if (l <= m) {
    unsigned a = __hip_atomic_fetch_add(p, zero, __ATOMIC_RELAXED, __HIP_MEMORY_SCOPE_AGENT), b;
    SB();
    __builtin_amdgcn_s_sleep(P_GAP);
#pragma unroll 1
    for (int it = 0; it < SPIN_G; ++it) {
      b = __hip_atomic_fetch_add(p, zero, __ATOMIC_RELAXED, __HIP_MEMORY_SCOPE_AGENT);
      SB();
      ++cnt;
      if (__all(a == tag)) break;
      a = __hip_atomic_fetch_add(p, zero, __ATOMIC_RELAXED, __HIP_MEMORY_SCOPE_AGENT);
      SB();
      ++cnt;
      if (__all(b == tag)) break;
    }
  }
  CBAR();
  return __builtin_amdgcn_readfirstlane(cnt);
}
__device__ __forceinline__ void issue4(rsrc_t rws, int off, int stride, v4u (&Q)[4]) {
#pragma unroll
  for (int t = 0; t < 4; ++t) Q[t] = ldg(rws, off, t * stride);
  SB();
}
__device__ __forceinline__ void check4(rsrc_t rws, int off, int stride, unsigned tag, v4u (&Q)[4]) {
#pragma unroll 1
  for (int it = 0; it < SPIN_G; ++it) {
    if (__all(tagged(Q[0], tag) & tagged(Q[1], tag) & tagged(Q[2], tag) & tagged(Q[3], tag))) break;
    CBAR();
    issue4(rws, off, stride, Q);
  }
}

#define ST(i) do {} while (0)
#define NP(v, e) (void)(e)

__global__ void __launch_bounds__(NTHR, 3) fused_decode(Params p) {
  __shared__ __attribute__((aligned(16))) char smem[L_BYTES];
  LAS char* L = (LAS char*)smem;
  const int tid = threadIdx.x, w = __builtin_amdgcn_readfirstlane(tid >> 6), l = tid & 63;
  const int c = blockIdx.x, x = c & 7, j = c >> 3;
  const int i16 = l & 15, g = l >> 4;
  ST(0);
  const int xp = x >> 1, xo = x & 1, t64 = xo * 32 + j;
  const int bb = 2 * xo + (j >> 4), sp = j & 15, pair = bb * 4 + xp;
  const int qks = c & 3, qcb = c >> 2;
  const int rb = 2 * x + (j >> 4), cb = sp;
  const bool big = xp < 2;
  const int rb0 = big ? 4 * xp : 8 + 3 * (xp - 2);
  const bool has_gu = big || j >= 8, has_dn = big || j < 16 || ((j >> 3) & 1) == xo;
  const int gt = big ? t64 : xo * 24 + j - 8, gsrc = gt & 3, gcb = rb0 * 4 + (gt >> 2);
  const int rbd = rb0 + (big ? (t64 >> 4) : (j < 16 ? xo : 2));
  const int k_wo = has_gu ? 20 : 12, k_dn = k_wo + 4;

  if (__builtin_expect(w == 9, 1)) {
    __builtin_amdgcn_s_setprio(3);
    int k = 0, unit = 0;
#define TILE(WN, r, v0, v1, v2, v3, sbase, istride) do { \
      constexpr int DD = (WN - 2) / 8, N0 = (WN - 2) % 8; \
      const rsrc_t r_ = (r); const int sb_ = (sbase), is_ = (istride); \
      _Pragma("unroll 1") for (int t = 0; t < 4; ++t) { \
        if (k >= NUNIT) { \
          if (k < NUNIT + 4) { _Pragma("unroll 1") for (int i = 0; i < SPIN_L; ++i) { const unsigned v = get_word(L, C_PJ); if ((v & 0x03030303u) && (v & 0xff) && (v & 0xff00) && (v & 0xff0000) && (v >> 24)) break; } } \
          else if (k < NUNIT + 8) wait_eq<1>(L, C_KD, ALL1, false); \
          else { wait_eq<1>(L, C_G2A, ALL1, false); wait_eq<1>(L, C_G2B, ALL1, false); } \
        } \
        const int so = sb_ + t * 8 * is_; \
        const unsigned m = (unsigned)(uintptr_t)L + unit * 8192; \
        _Pragma("unroll") for (int n = 0; n < 8; ++n) { \
          asm volatile("s_mov_b32 m0, %0\n\ts_nop 0\n\tbuffer_load_dwordx4 %1, %2, %3 offen" NT_STR " lds\n\ts_waitcnt vmcnt(" STR(WN - 1) ")" \
                       :: "s"(m + n * 1024), "v"((n & 3) == 0 ? (v0) : (n & 3) == 1 ? (v1) : (n & 3) == 2 ? (v2) : (v3)), "s"(r_), "s"(so + n * is_) : "memory"); \
          if (n == N0 && k > DD) set_word(C_PROG, (unsigned)(k - DD)); \
        } \
        if (k == 0) __builtin_amdgcn_s_barrier(); \
        ++k; if (++unit == NUNIT) unit = 0; \
      } } while (0)
    const int r8 = l >> 3, ce = ((l & 7) ^ (r8 & 2)) * 16, co = ce ^ 64;
    const int kvo = (pair * 8192 + 6144 + sp * 128) * 256;
    const int kb = g * 256 + ((i16 ^ g) * 16), vb = g * 256 + ((i16 ^ (2 * g)) * 16);
#pragma unroll 1
    for (int tl = 0; tl < 7; ++tl) {
      if ((tl == 3 || tl == 4) && !has_gu) continue;
      if (tl == 6 && !has_dn) continue;
      const u16* base = tl == 0 ? p.wq : tl == 1 ? p.kc : tl == 2 ? p.vc : tl < 5 ? ((gsrc & 2) ? p.wu : p.wg) : tl == 5 ? p.wo : p.wd;
      const int bytes = tl == 0 || tl == 5 ? 8 << 20 : tl < 3 ? 32 << 20 : 7 << 20;
      const int ld = tl == 0 ? 8192 : tl < 3 ? 128 : tl < 5 ? 7168 : 2048;
      const int sb = tl == 0 ? qks * 256 * 8192 + qcb * 128 : tl < 3 ? kvo : tl < 5 ? ((gsrc & 1) * 512 + (tl - 3) * 256) * 7168 + gcb * 128
                   : (tl == 5 ? rb : rbd) * 256 * 2048 + cb * 128;
      const bool kv = tl == 1 || tl == 2;
      const int v0 = tl == 1 ? kb : tl == 2 ? vb : r8 * ld + ce;
      const int x1 = tl == 1 ? 64 : tl == 2 ? 128 : 64, x2 = tl == 1 ? 128 : 0;
      (void)kv; (void)co;
      if (WIN1 == WIN2 || tl < 5) TILE(WIN1, mk(base, bytes), v0, v0 ^ x1, v0 ^ x2, v0 ^ x1 ^ x2, sb, 8 * ld);
      else TILE(WIN2, mk(base, bytes), v0, v0 ^ x1, v0 ^ x2, v0 ^ x1 ^ x2, sb, 8 * ld);
    }
    if ((WIN2 - 2) / 8 >= 3) { asm volatile("s_waitcnt vmcnt(24)" ::: "memory"); set_word(C_PROG, (unsigned)(k - 3)); }
    if ((WIN2 - 2) / 8 >= 2) { asm volatile("s_waitcnt vmcnt(16)" ::: "memory"); set_word(C_PROG, (unsigned)(k - 2)); }
    asm volatile("s_waitcnt vmcnt(8)" ::: "memory"); set_word(C_PROG, (unsigned)(k - 1));
    asm volatile("s_waitcnt vmcnt(0)" ::: "memory"); set_word(C_PROG, (unsigned)k);
    ST(8);
  } else if (w < 8) {
    const rsrc_t rws = mk(p.ws, WS_END);
    char* mb = p.ws + WS_MB + c * 1024;
    const int w4 = w & 3, q4 = i16 >> 2, pp = l & 3;
    const int wa = (8 * g + q4) * 128 + (((2 * w4 + (pp >> 1)) ^ (2 * (((q4 >> 1) & 1) | ((g & 1) << 1)))) * 16) + 8 * (pp & 1);
    unsigned tag = 0, prog = 0;
    if (w >= 4) {
      const rsrc_t r = mk(p.x, 4 * 1024 * 2);
      const v4u a = __builtin_amdgcn_raw_buffer_load_b128(r, w4 * 2048 + l * 16, 0, 0), b = __builtin_amdgcn_raw_buffer_load_b128(r, w4 * 2048 + 1024 + l * 16, 0, 0);
      SB();
      __builtin_amdgcn_s_barrier();
      CBAR();
      SB();
      float v[16];
#pragma unroll
      for (int n = 0; n < 4; ++n) { v[2 * n] = bflo(a[n]); v[2 * n + 1] = bfhi(a[n]); v[8 + 2 * n] = bflo(b[n]); v[9 + 2 * n] = bfhi(b[n]); }
      float s = 0.f;
#pragma unroll
      for (int n = 0; n < 16; ++n) s += v[n];
      const float mean = wave_sum(s) * (1.f / 1024.f);
      float q = 0.f;
#pragma unroll
      for (int n = 0; n < 16; ++n) { const float d = v[n] - mean; q = fmaf(d, d, q); }
      const float var = rbf(wave_sum(q) * (1.f / 1024.f));
      const float mb_ = rbf(mean);
      const float rs = __builtin_amdgcn_rcpf(rbf(__builtin_amdgcn_sqrtf(rbf(var + 1e-5f))));
      v4u oa, ob;
#pragma unroll
      for (int n = 0; n < 4; ++n) {
        const unsigned da = pk(v[2 * n] - mb_, v[2 * n + 1] - mb_), db = pk(v[8 + 2 * n] - mb_, v[9 + 2 * n] - mb_);
        oa[n] = pk(bflo(da) * rs, bfhi(da) * rs); ob[n] = pk(bflo(db) * rs, bfhi(db) * rs);
      }
      *(LAS v4u*)(L + L_XN + w4 * 2048 + l * 16) = oa;
      *(LAS v4u*)(L + L_XN + w4 * 2048 + 1024 + l * 16) = ob;
      set_byte(C_XN, w4, 1u);
      ST(1);
    } else {
      __builtin_amdgcn_s_barrier();
      CBAR();
    }
#pragma unroll 1
    for (int pass = 0; pass < 2; ++pass) {
      int dryi = DRY ? 1 - pass : 0; asm volatile("" : "+s"(dryi));
      const bool dry = dryi != 0;
      if (!DRY && pass == 0 && w < 4) continue;
      const unsigned one = dry ? 0u : 1u;
      if (w < 4) {
        wait_eq<2>(L, C_TAGV, 1u, dry);
        tag = get_word(L, C_TAG);
        const int qoff = WS_GQ + (bb * 2048 + xp * 512 + w * 128 + l * 2) * 8;
        const int h = 2 * w + (l >> 5);
        int qw = L_QS + h * 256 + ((((l & 31) >> 1) ^ (2 * h)) * 16) + (l & 1) * 8;
        int qr = L_QS + (i16 & 7) * 256 + ((g ^ (2 * (i16 & 7))) * 16);
        int ka = (4 + w) * 8192 + i16 * 256 + ((g ^ i16) * 16);
        PINV(qw); PINV(qr); PINV(ka);
        wait_prog<2>(L, prog, 4, dry);
        ST(1);
        v4u Q[4] = {};
        if (!dry) { NP(np0, poll_words(mb + MB_Q + w * 64, 15, tag, l)); ST(2); issue4(rws, qoff, 4 * 2048 * 8, Q); }
        wait_prog<1>(L, prog, 5 + w, dry);
        v4u KF[8];
#pragma unroll
        for (int n = 0; n < 8; ++n) KF[n] = *(LAS v4u*)(L + ((ka ^ ((n & 3) * 64)) + (n >> 2) * 4096));
        set_byte(C_KD, w, one);
        SB();
        if (!dry) check4(rws, qoff, 4 * 2048 * 8, tag, Q);
        ST(3);
        const float a0 = (bflo(Q[0][0]) + bflo(Q[1][0])) + (bflo(Q[2][0]) + bflo(Q[3][0]));
        const float a1 = (bfhi(Q[0][0]) + bfhi(Q[1][0])) + (bfhi(Q[2][0]) + bfhi(Q[3][0]));
        const float a2 = (bflo(Q[0][2]) + bflo(Q[1][2])) + (bflo(Q[2][2]) + bflo(Q[3][2]));
        const float a3 = (bfhi(Q[0][2]) + bfhi(Q[1][2])) + (bfhi(Q[2][2]) + bfhi(Q[3][2]));
        *(LAS v2u*)(L + qw) = v2u{pk(a0, a1), pk(a2, a3)};
        set_byte(C_QS, w, one);
        wait_eq<0>(L, C_QS, ALL1, dry);
        v4u qf[4];
#pragma unroll
        for (int s = 0; s < 4; ++s) qf[s] = *(LAS v4u*)(L + (qr ^ (s * 64)));
        f32x4 d0 = Z4, d1 = Z4;
#pragma unroll
        for (int s = 0; s < 4; ++s) { d0 = mfma(KF[s], qf[s], d0); d1 = mfma(KF[4 + s], qf[s], d1); }
        float sc[8] = {d0[0], d0[1], d0[2], d0[3], d1[0], d1[1], d1[2], d1[3]};
#pragma unroll
        for (int n = 0; n < 8; n += 2) {
          const unsigned a = pk(sc[n], sc[n + 1]);
          const unsigned b = pk(bflo(a) * 0.08838834764831845f, bfhi(a) * 0.08838834764831845f);
          sc[n] = bflo(b); sc[n + 1] = bfhi(b);
        }
        float m = __builtin_fmaxf(__builtin_fmaxf(__builtin_fmaxf(sc[0], sc[1]), sc[2]), __builtin_fmaxf(__builtin_fmaxf(sc[3], sc[4]), sc[5]));
        m = __builtin_fmaxf(__builtin_fmaxf(m, sc[6]), sc[7]);
        m = xrow_max(m) * 1.4426950408889634f;
#pragma unroll
        for (int n = 0; n < 8; ++n) sc[n] = __builtin_amdgcn_exp2f(fmaf(sc[n], 1.4426950408889634f, -m));
        *(LAS v4u*)(L + L_PS + w * 1024 + l * 16) = v4u{pk(sc[0], sc[1]), pk(sc[2], sc[3]), pk(sc[4], sc[5]), pk(sc[6], sc[7])};
        const float ls = xrow_sum(((sc[0] + sc[1]) + (sc[2] + sc[3])) + ((sc[4] + sc[5]) + (sc[6] + sc[7])));
        *(LAS f2_t*)(L + L_ML + i16 * 32 + w * 8) = f2_t{m, ls};
        set_byte(C_P, w, one);
        ST(4);
      }
      if (w < 4 || pass == 1 || true) {
      }
      if (w >= 4 && pass == 0) {
      }
      if (w < 4 || pass == 1) {
        const int rr = 4 * g + q4;
        int va = 8 * 8192 + rr * 256 + (((2 * w + (pp >> 1)) ^ (2 * (rr & 7))) * 16) + 8 * (pp & 1);
        int goff = i16 < 8 && !dry ? WS_G2 + ((pair * 16 + 2 * i16 + (w >> 2)) * 16 + sp) * 384 + w4 * 64 + g * 16 : OOB;
        int loff = l < 8 && w4 == 0 && !dry ? goff + 256 : OOB;
        int noff = l < 8 && !dry ? WS_MB + (((j & 16) + 2 * l + (w >> 2)) * 8 + x) * 1024 + MB_2 + (sp * 4 + w4) * 4 : OOB;
        PINV(va); PINV(goff); PINV(loff); PINV(noff);
        v4u VF[4];
#pragma unroll
        for (int t = 0; t < 3; ++t) { wait_prog<1>(L, prog, 9 + t, dry); VF[t] = trfrag(L + va + t * 8192, 4096); }
        wait_eq<0>(L, C_P, ALL1, dry);
        v4u PF[4];
#pragma unroll
        for (int t = 0; t < 4; ++t) PF[t] = *(LAS v4u*)(L + L_PS + t * 1024 + l * 16);
        const f32x4 ml0 = *(LAS f32x4*)(L + L_ML + i16 * 32), ml1 = *(LAS f32x4*)(L + L_ML + i16 * 32 + 16);
        const f32x4 A0 = mfma(VF[0], PF[0], Z4), A1 = mfma(VF[1], PF[1], Z4), A2 = mfma(VF[2], PF[2], Z4);
        const float M = vmax(vmax(ml0[0], ml0[2]), vmax(ml1[0], ml1[2]));
        float a0 = __builtin_amdgcn_exp2f(ml0[0] - M), a1 = __builtin_amdgcn_exp2f(ml0[2] - M), a2 = __builtin_amdgcn_exp2f(ml1[0] - M), a3 = __builtin_amdgcn_exp2f(ml1[2] - M);
        const float Ls = (a0 * ml0[1] + a1 * ml0[3]) + (a2 * ml1[1] + a3 * ml1[3]);
        const float inv = __builtin_amdgcn_rcpf(Ls);
        float lse = M + __builtin_amdgcn_logf(Ls);
        f32x4 ov = (A0 * a0 + A1 * a1 + A2 * a2) * inv;
        a3 *= inv;
        PINV(ov); PINV(a3); PINV(lse);
        ST(5);
        wait_prog<0>(L, prog, 12, dry);
        ov += mfma(trfrag(L + va + 3 * 8192, 4096), PF[3], Z4) * a3;
        stg(rws, goff, v4u{pk(ov[0], ov[1]), tag, pk(ov[2], ov[3]), tag});
        stg2(rws, loff, v2u{asu(lse), tag});
        stg1(rws, noff, tag);
        set_byte(C_G2A + (w >> 2), w4, one);
        ST(6);
      }
      if (w >= 4) {
        const bool job = pass == 1;
        if (!job) { wait_eq<1>(L, C_XN, ALL1, false); wait_eq<1>(L, C_TAGV, 1u, false); tag = get_word(L, C_TAG); }
        if (!job || has_gu) {
          const int k0 = job ? 12 : 0, nt = job ? 8 : 4;
          LAS char* xa = L + L_XN + pp * 2048 + ((job ? (gsrc & 1) * 512 : qks * 256) + g * 8) * 2;
          int soff = i16 >= 4 ? OOB : job ? WS_GGU + ((gsrc * 4 + pp) * 1792 + gcb * 32 + w4 * 8 + g * 2) * 8 : WS_GQ + ((qks * 4 + pp) * 2048 + qcb * 32 + w4 * 8 + g * 2) * 8;
          const int rl = gt >> 4;
          const int gj = big ? (rl & 1) * 16 + l : (rl < 2 ? l : 16 + l), gx = big ? rl >> 1 : (rl < 2 ? rl : (l >> 3) & 1);
          int noff = job ? (l < 16 ? WS_MB + (gj * 8 + 2 * xp + gx) * 1024 + MB_G + (gt & 15) * 4 : OOB)
                         : WS_MB + ((l & 31) * 8 + 2 * (qcb >> 4) + (l >> 5)) * 1024 + MB_Q + ((qcb & 15) * 4 + qks) * 4;
          PINV(soff); PINV(noff);
          f32x4 acc0 = Z4, acc1 = Z4;
#pragma unroll 1
          for (int t = 0; t < nt; ++t) {
            const v4u x0 = *(LAS v4u*)(xa + t * 128), x1 = *(LAS v4u*)(xa + t * 128 + 64);
            LAS char* u = L + unit_of(k0 + t) + wa;
            wait_prog<1>(L, prog, k0 + t + 1, false);
            acc0 = mfma(trfrag(u, 512), x0, acc0);
            acc1 = mfma(trfrag(u + 4096, 512), x1, acc1);
          }
          if (job) ST(7); else ST(2);
          acc0 += acc1;
          stg(rws, soff, v4u{pk(acc0[0], acc0[1]), tag, pk(acc0[2], acc0[3]), tag});
          set_byte(C_PJ, w4, job ? 2u : 1u);
          if (w4 == 0) { wait_eq<0>(L, C_PJ, job ? ALL2 : ALL1, false); stg1(rws, noff, tag); }
          if (job) ST(8); else ST(3);
        } else wait_prog<4>(L, prog, k_dn + 4, false);
        if (has_dn) {
          const int hoff = WS_GGU + (g * 1792 + rbd * 128 + w4 * 32 + i16 * 2) * 8;
          v4u H[4] = {};
          if (job) {
            NP(np0, poll_words(mb + MB_G + w4 * 16, 3, tag, l));
            ST(9);
            issue4(rws, hoff, 4 * 1792 * 8, H);
            check4(rws, hoff, 4 * 1792 * 8, tag, H);
            ST(10);
          }
          const unsigned g0 = pk(bflo(H[0][0]) + bflo(H[1][0]), bfhi(H[0][0]) + bfhi(H[1][0])), g1 = pk(bflo(H[0][2]) + bflo(H[1][2]), bfhi(H[0][2]) + bfhi(H[1][2]));
          const unsigned u0 = pk(bflo(H[2][0]) + bflo(H[3][0]), bfhi(H[2][0]) + bfhi(H[3][0])), u1 = pk(bflo(H[2][2]) + bflo(H[3][2]), bfhi(H[2][2]) + bfhi(H[3][2]));
          *(LAS v2u*)(L + L_HF + g * 528 + w4 * 128 + i16 * 8) = v2u{pk(bflo(g0) * bflo(u0), bfhi(g0) * bfhi(u0)), pk(bflo(g1) * bflo(u1), bfhi(g1) * bfhi(u1))};
          set_byte(C_HF, w4, job ? 1u : 0u);
        }
      } else {
        int aa = pp * 528 + g * 16;
        const bool u1 = i16 & 4;
        int soff = i16 < 8 && !dry ? WS_G5 + ((cb * 16 + pp * 4 + w) * 16 + rb) * 128 + g * 32 + (u1 ? 16 : 0) : OOB;
        int noff = l < 4 && !dry ? WS_MB + (((w & 1) * 16 + cb) * 8 + 2 * l + (w >> 1)) * 1024 + MB_5 + rb * 4 : OOB;
        PINV(aa); PINV(soff); PINV(noff);
        v4u WF[8], DF[8];
#pragma unroll
        for (int t = 0; t < 4; ++t) {
          LAS char* u = L + unit_of(k_wo + t) + wa;
          wait_prog<1>(L, prog, k_wo + t + 1, dry);
          WF[2 * t] = trfrag(u, 512); WF[2 * t + 1] = trfrag(u + 4096, 512);
        }
        if (has_dn) {
#pragma unroll
          for (int t = 0; t < 3; ++t) {
            LAS char* u = L + unit_of(k_dn + t) + wa;
            wait_prog<1>(L, prog, k_dn + t + 1, dry);
            DF[2 * t] = trfrag(u, 512); DF[2 * t + 1] = trfrag(u + 4096, 512);
          }
        }
        ST(7);
        f32x4 acc0 = Z4, acc1 = Z4;
        wait_eq<0>(L, C_OF, 1u, dry);
#pragma unroll
        for (int s = 0; s < 8; s += 2) { acc0 = mfma(WF[s], *(LAS v4u*)(L + L_OF + aa + s * 64), acc0); acc1 = mfma(WF[s + 1], *(LAS v4u*)(L + L_OF + aa + s * 64 + 64), acc1); }
        ST(8);
        if (has_dn) {
          LAS char* u = L + unit_of(k_dn + 3) + wa;
          wait_eq<0>(L, C_HF, ALL1, dry);
          v4u hf[8];
#pragma unroll
          for (int s = 0; s < 8; ++s) hf[s] = *(LAS v4u*)(L + L_HF + aa + s * 64);
#pragma unroll
          for (int s = 0; s < 6; s += 2) { acc0 = mfma(DF[s], hf[s], acc0); acc1 = mfma(DF[s + 1], hf[s + 1], acc1); }
          ST(9);
          wait_prog<0>(L, prog, k_dn + 4, dry);
          acc0 = mfma(trfrag(u, 512), hf[6], acc0);
          acc1 = mfma(trfrag(u + 4096, 512), hf[7], acc1);
        }
        acc0 += acc1;
        stg(rws, soff, v4u{asu(u1 ? acc0[2] : acc0[0]), tag, asu(u1 ? acc0[3] : acc0[1]), tag});
        stg1(rws, noff, tag);
        set_byte(C_G5, w, one);
        ST(10);
      }
    }
  } else {
    __builtin_amdgcn_s_setprio(2);
    const rsrc_t rws = mk(p.ws, WS_END);
    char* mb = p.ws + WS_MB + c * 1024;
    const unsigned ev = __builtin_amdgcn_raw_buffer_load_b32(rws, WS_E, 0, AUX_SC1);
    const unsigned warm = __builtin_amdgcn_raw_buffer_load_b32(rws, WS_END - 4, 0, AUX_SC1);
    const int oidx = (rb >> 2) * 1024 + cb * 64 + (rb & 3) * 16 + (l & 7) * 2;
    const unsigned xres = *(const unsigned*)(p.x + oidx);
    if (l < 16) *((LAS unsigned*)(L + L_CNT) + l) = 0u;
    LGKM0();
    SB();
    __builtin_amdgcn_s_barrier();
    CBAR();
    SB();
    const unsigned tag = __builtin_amdgcn_readfirstlane(ev) + 1u;
    set_word(C_TAG, tag);
    set_word(C_TAGV, 1u);
    asm volatile("" :: "v"(warm));
    const rsrc_t rout = mk(p.out, 8192);
#pragma unroll 1
    for (int pass = DRY ? 0 : 1; pass < 2; ++pass) {
      int dryi = 1 - pass; asm volatile("" : "+s"(dryi));
      const bool dry = dryi != 0;
      const int off2 = WS_G2 + ((pair * 16 + sp) * 16 + g) * 384;
      int soff = l < 16 && !dry ? WS_GO + (bb * 2048 + xp * 512 + sp * 32 + l * 2) * 8 : OOB;
      int noff = l < 16 && !dry ? WS_MB + ((((sp >> 2) & 1) * 16 + l) * 8 + 2 * xp + (sp >> 3)) * 1024 + MB_O + (bb * 4 + (sp & 3)) * 4 : OOB;
      int ooff = l < 8 && !dry ? oidx * 2 : OOB;
      PINV(soff); PINV(noff); PINV(ooff);
      wait_eq<2>(L, C_G2A, ALL1, dry);
      ST(1);
      v4u P[4] = {}; v2u S[4] = {};
      if (!dry) {
        NP(np0, poll_words(mb + MB_2, 63, tag, l));
        ST(2);
#pragma unroll 1
        for (int it = 0; it < SPIN_G; ++it) {
          CBAR();
          bool ok = true;
#pragma unroll
          for (int t = 0; t < 4; ++t) { P[t] = ldg(rws, off2 + i16 * 16, t * 4 * 384); S[t] = ldg2(rws, off2 + 256, t * 4 * 384); }
#pragma unroll
          for (int t = 0; t < 4; ++t) ok &= tagged(P[t], tag) & (S[t][1] == tag);
          if (__all(ok)) break;
        }
      }
      ST(3);
      const float M = xrow_max(vmax(vmax(asf(S[0][0]), asf(S[1][0])), vmax(asf(S[2][0]), asf(S[3][0]))));
      float ws = 0.f, o0 = 0.f, o1 = 0.f, o2 = 0.f, o3 = 0.f;
#pragma unroll
      for (int t = 0; t < 4; ++t) {
        const float a = __builtin_amdgcn_exp2f(asf(S[t][0]) - M);
        ws += a; o0 = fmaf(a, bflo(P[t][0]), o0); o1 = fmaf(a, bfhi(P[t][0]), o1); o2 = fmaf(a, bflo(P[t][2]), o2); o3 = fmaf(a, bfhi(P[t][2]), o3);
      }
      const float inv = __builtin_amdgcn_rcpf(xrow_sum(ws));
      o0 = xrow_sum(o0) * inv; o1 = xrow_sum(o1) * inv; o2 = xrow_sum(o2) * inv; o3 = xrow_sum(o3) * inv;
      stg(rws, soff, v4u{pk(o0, o1), tag, pk(o2, o3), tag});
      stg1(rws, noff, tag);
      ST(4);
      v4u O[4] = {};
      if (!dry) {
        NP(np1, poll_words(mb + MB_O, 15, tag, l));
        ST(5);
        issue4(rws, WS_GO + (rb * 128 + l * 2) * 8, 2048 * 8, O);
        check4(rws, WS_GO + (rb * 128 + l * 2) * 8, 2048 * 8, tag, O);
      }
#pragma unroll
      for (int t = 0; t < 4; ++t) *(LAS v2u*)(L + L_OF + t * 528 + l * 8) = v2u{O[t][0], O[t][2]};
      set_word(C_OF, dry ? 0u : 1u);
      ST(6);
      wait_eq<1>(L, C_G5, ALL1, dry);
      ST(7);
      v4u P0 = {}, P1 = {};
      if (!dry) {
        NP(np2, poll_words(mb + MB_5, 15, tag, l));
        ST(8);
        const int off = WS_G5 + ((cb * 16 + rb) * 256 + l * 2) * 8;
#pragma unroll 1
        for (int it = 0; it < SPIN_G; ++it) {
          CBAR();
          P0 = ldg(rws, off, 0); P1 = ldg(rws, off, 1024);
          if (__all(tagged(P0, tag) & tagged(P1, tag))) break;
        }
      }
      ST(9);
      float a = asf(P0[0]) + asf(P1[0]), b = asf(P0[2]) + asf(P1[2]);
      a += dpp<ROR8>(a); b += dpp<ROR8>(b);
      a = xrow_sum(a); b = xrow_sum(b);
      __builtin_amdgcn_raw_buffer_store_b32(pk(bflo(xres) + a, bfhi(xres) + b), rout, ooff, 0, 0);
      stg1(rws, c == 0 && l == 0 && !dry ? WS_E : OOB, tag);
      ST(10);
    }
  }
}

__global__ void __launch_bounds__(64) probe_k(unsigned* sink) {
  const unsigned long long t0 = __builtin_amdgcn_s_memrealtime();
  __shared__ unsigned pl[39600];
  while (__builtin_amdgcn_s_memrealtime() - t0 < 1000ull) __builtin_amdgcn_s_sleep(2);

  if (t0 == 12345ull) { pl[threadIdx.x] = 1; __syncthreads(); sink[0] = pl[(threadIdx.x + 1) % 64]; }
}
extern "C" void kernel_launch(void* const* d_in, const int* in_sizes, int n_in, void* d_out, int out_size, void* d_ws, size_t ws_size, hipStream_t stream) {
  Params p;
  p.x = (const u16*)d_in[0]; p.kc = (const u16*)d_in[1]; p.vc = (const u16*)d_in[2]; p.wq = (const u16*)d_in[3]; p.wo = (const u16*)d_in[4];
  p.wg = (const u16*)d_in[6]; p.wu = (const u16*)d_in[7]; p.wd = (const u16*)d_in[8]; p.out = (u16*)d_out; p.ws = (char*)d_ws;
  probe_k<<<256, 64, 0, stream>>>((unsigned*)((char*)d_ws + (64 << 20)));
  fused_decode<<<256, NTHR, 0, stream>>>(p);
}
```

```cpp
#include <hip/hip_runtime.h>
#include <stdint.h>

#define WIN 26
#define POL " sc1 nt"
constexpr int TQ = 9, T2G = 21, TH = 22;

typedef unsigned v4u __attribute__((ext_vector_type(4)));
typedef unsigned v2u __attribute__((ext_vector_type(2)));
typedef float f32x4 __attribute__((ext_vector_type(4)));
typedef short bf16x8 __attribute__((ext_vector_type(8)));
typedef short s16x4 __attribute__((ext_vector_type(4)));
typedef unsigned short u16;
#define LAS __attribute__((address_space(3)))
typedef __amdgpu_buffer_rsrc_t rsrc_t;

constexpr int NTHR = 640;
constexpr int WS_E = 0;
constexpr int WS_G2L = 4096;
constexpr int WS_GQ = WS_G2L + 256 * 64;
constexpr int WS_GGU = WS_GQ + 4 * 4 * 4096 * 8;
constexpr int WS_G5 = WS_GGU + 8 * 4 * 1792 * 8;
constexpr int WS_G2 = WS_G5 + 4 * 64 * 64 * 64;
constexpr int WS_END = WS_G2 + 16 * 16 * 16 * 256;
constexpr int NUNIT = 17;
constexpr int L_XN = NUNIT * 8192;
constexpr int L_QS = L_XN + 8192;
constexpr int L_PS = L_QS + 2048;
constexpr int L_OF = L_PS + 4096;
constexpr int L_HF = L_OF + 720;
constexpr int L_ML = L_HF + 720;
constexpr int L_CNT = L_ML + 512;
constexpr int L_BYTES = L_CNT + 64;
static_assert(L_BYTES <= 160 * 1024, "LDS");
enum { C_PROG = 0, C_HF = 1, C_OF = 2, C_P = 3, C_PJ = 4, C_KD = 5, C_GD = 6, C_QS = 7, C_XN = 8, C_TAG = 9, C_G5 = 10, C_TAGV = 11 };
constexpr unsigned ALL1 = 0x01010101u;
constexpr int SPIN_G = 1 << 14, SPIN_L = 1 << 20;
constexpr int AUX_SC1 = 16;

struct Params { const u16 *wq, *x; char* ws; const u16 *kc, *vc, *wg, *wu, *wo, *wd; u16* out; };

__device__ __forceinline__ rsrc_t mk(const void* p, int bytes) { return __builtin_amdgcn_make_buffer_rsrc((void*)p, 0, bytes, 0x00020000); }
__device__ __forceinline__ v4u ldg(rsrc_t r, int voff, int soff) { return __builtin_amdgcn_raw_buffer_load_b128(r, voff, soff, AUX_SC1); }
__device__ __forceinline__ v2u ldg2(rsrc_t r, int voff) { return __builtin_amdgcn_raw_buffer_load_b64(r, voff, 0, AUX_SC1); }
__device__ __forceinline__ void stg(rsrc_t r, int voff, v4u v) { __builtin_amdgcn_raw_buffer_store_b128(v, r, voff, 0, AUX_SC1); }
__device__ __forceinline__ void stg2(rsrc_t r, int voff, v2u v) { __builtin_amdgcn_raw_buffer_store_b64(v, r, voff, 0, AUX_SC1); }
__device__ __forceinline__ void stg1(rsrc_t r, int voff, unsigned v) { __builtin_amdgcn_raw_buffer_store_b32(v, r, voff, 0, AUX_SC1); }
constexpr int OOB = 0x7ffffff0;
__device__ __forceinline__ float asf(unsigned u) { return __builtin_bit_cast(float, u); }
__device__ __forceinline__ unsigned asu(float f) { return __builtin_bit_cast(unsigned, f); }
__device__ __forceinline__ float bflo(unsigned w) { return asf(w << 16); }
__device__ __forceinline__ float bfhi(unsigned w) { return asf(w & 0xffff0000u); }
typedef __bf16 bf2_t __attribute__((ext_vector_type(2)));
typedef float f2_t __attribute__((ext_vector_type(2)));
__device__ __forceinline__ unsigned pk(float lo, float hi) { f2_t f = {lo, hi}; return __builtin_bit_cast(unsigned, __builtin_convertvector(f, bf2_t)); }
__device__ __forceinline__ float rbf(float f) { return bflo(pk(f, f)); }
__device__ __forceinline__ f32x4 mfma(v4u a, v4u b, f32x4 c) { return __builtin_amdgcn_mfma_f32_16x16x32_bf16(__builtin_bit_cast(bf16x8, a), __builtin_bit_cast(bf16x8, b), c, 0, 0, 0); }
template <int CTRL> __device__ __forceinline__ float dpp(float x) { return asf(__builtin_amdgcn_mov_dpp((int)asu(x), CTRL, 0xf, 0xf, true)); }
constexpr int XOR1 = 0xB1, XOR2 = 0x4E, XOR7 = 0x141, ROR8 = 0x128, ROR4 = 0x124;
__device__ __forceinline__ float vmax(float a, float b) { return __builtin_fmaxf(a, b); }
__device__ __forceinline__ float xrow_max(float x) {
  auto s = __builtin_amdgcn_permlane16_swap(asu(x), asu(x), false, false); x = vmax(asf(s[0]), asf(s[1]));
  auto t = __builtin_amdgcn_permlane32_swap(asu(x), asu(x), false, false); return vmax(asf(t[0]), asf(t[1]));
}
__device__ __forceinline__ float xrow_sum(float x) {
  auto s = __builtin_amdgcn_permlane16_swap(asu(x), asu(x), false, false); x = asf(s[0]) + asf(s[1]);
  auto t = __builtin_amdgcn_permlane32_swap(asu(x), asu(x), false, false); return asf(t[0]) + asf(t[1]);
}
__device__ __forceinline__ float wave_sum(float x) { x += dpp<XOR1>(x); x += dpp<XOR2>(x); x += dpp<XOR7>(x); x += dpp<ROR8>(x); return xrow_sum(x); }
#define SB() __builtin_amdgcn_sched_barrier(0)
#define BARRIER() asm volatile("s_waitcnt lgkmcnt(0)\n\ts_barrier" ::: "memory")
#define CBAR() asm volatile("" ::: "memory")
#define PINV(v) asm volatile("" : "+v"(v))
#define STR2(x) #x
#define STR(x) STR2(x)
constexpr f32x4 Z4 = {0.f, 0.f, 0.f, 0.f};

__device__ __forceinline__ void set_byte(int c, int i, unsigned v) {
  asm volatile("s_mov_b64 exec, 1\n\tds_write_b8 %0, %1\n\ts_mov_b64 exec, -1" :: "v"(L_CNT + c * 4 + i), "v"(v) : "memory");
}
__device__ __forceinline__ void set_word(int c, unsigned v) {
  asm volatile("s_mov_b64 exec, 1\n\tds_write_b32 %0, %1\n\ts_mov_b64 exec, -1" :: "v"(L_CNT + c * 4), "v"(v) : "memory");
}
__device__ __forceinline__ unsigned get_word(LAS char* L, int c) {
  const unsigned v = __builtin_amdgcn_readfirstlane(__hip_atomic_load((LAS unsigned*)(L + L_CNT) + c, __ATOMIC_RELAXED, __HIP_MEMORY_SCOPE_WORKGROUP));
  CBAR();
  return v;
}
template <int SLEEP> __device__ __forceinline__ void wait_eq(LAS char* L, int c, unsigned want) {
#pragma unroll 1
  for (int i = 0; i < SPIN_L; ++i) {
    if (get_word(L, c) == want) break;
    if (SLEEP) __builtin_amdgcn_s_sleep(SLEEP);
  }
}
template <int SLEEP> __device__ __forceinline__ void wait_prog(LAS char* L, unsigned& prog, int want) {
  if (prog < (unsigned)want) {
#pragma unroll 1
    for (int i = 0; i < SPIN_L; ++i) {
      prog = get_word(L, C_PROG);
      if (prog >= (unsigned)want) break;
      if (SLEEP) __builtin_amdgcn_s_sleep(SLEEP);
    }
  }
  CBAR();
}
__device__ __forceinline__ int unit_of(int k) { return (k >= NUNIT + 8 ? k - 13 : k >= NUNIT ? k - NUNIT : k) * 8192; }
__device__ __forceinline__ v4u trfrag(LAS char* a0, int d) {
  const s16x4 lo = __builtin_amdgcn_ds_read_tr16_b64_v4i16((LAS s16x4*)a0), hi = __builtin_amdgcn_ds_read_tr16_b64_v4i16((LAS s16x4*)(a0 + d));
  const v2u a = __builtin_bit_cast(v2u, lo), b = __builtin_bit_cast(v2u, hi);
  return v4u{a[0], a[1], b[0], b[1]};
}
__device__ __forceinline__ v4u trfrag2(LAS char* a0, LAS char* a1) {
  const s16x4 lo = __builtin_amdgcn_ds_read_tr16_b64_v4i16((LAS s16x4*)a0), hi = __builtin_amdgcn_ds_read_tr16_b64_v4i16((LAS s16x4*)a1);
  const v2u a = __builtin_bit_cast(v2u, lo), b = __builtin_bit_cast(v2u, hi);
  return v4u{a[0], a[1], b[0], b[1]};
}
__device__ __forceinline__ bool tagged(v4u v, unsigned tag) { return (v[1] == tag) & (v[3] == tag); }
__device__ __forceinline__ void issue4(rsrc_t rws, int off, int stride, v4u (&Q)[4]) {
#pragma unroll
  for (int t = 0; t < 4; ++t) Q[t] = ldg(rws, off, t * stride);
  SB();
}
__device__ __forceinline__ void check4(rsrc_t rws, int off, int stride, unsigned tag, v4u (&Q)[4]) {
#pragma unroll 1
  for (int it = 0; it < SPIN_G; ++it) {
    const bool ok = tagged(Q[0], tag) & tagged(Q[1], tag) & tagged(Q[2], tag) & tagged(Q[3], tag);
    if (__all(ok)) break;
    CBAR();
    if (!ok) issue4(rws, off, stride, Q);
  }
}
__device__ __forceinline__ void check8(rsrc_t rws, int off, int off2, int stride, unsigned tag, unsigned tag2, v4u (&Q)[4], v4u (&R)[4]) {
#pragma unroll 1
  for (int it = 0; it < SPIN_G; ++it) {
    const bool ok = tagged(Q[0], tag) & tagged(Q[1], tag) & tagged(Q[2], tag) & tagged(Q[3], tag), ok2 = tagged(R[0], tag2) & tagged(R[1], tag2) & tagged(R[2], tag2) & tagged(R[3], tag2);
    if (__all(ok & ok2)) break;
    CBAR();
    if (!ok) issue4(rws, off, stride, Q);
    if (!ok2) issue4(rws, off2, stride, R);
  }
}
__device__ __forceinline__ void wait_pos(LAS char* L, unsigned& prog, int t, int ktot) {
  wait_prog<2>(L, prog, t < ktot ? t : ktot);
#pragma unroll 1
  for (int i = ktot; i < t; ++i) __builtin_amdgcn_s_sleep(12);
}

#define PAD(n) asm volatile("s_branch .Lfd_skip" #n "\n.Lfd_pad" #n ":\n\ts_setpc_b64 s[90:91]\n.Lfd_skip" #n ":" ::: "memory")
#define PADV(n, v) asm volatile("s_branch .Lfd_skip" #n "\n.Lfd_pad" #n ":\n\ts_setpc_b64 s[90:91]\n.Lfd_skip" #n ":" : "+v"(v))
#define PADA(n) asm volatile("s_branch .Lfd_skip" #n "\n\t.p2align 12\n.Lfd_pad" #n ":\n\ts_setpc_b64 s[90:91]\n.Lfd_skip" #n ":" ::: "memory")
#define TB(n) "s_branch .Lfd_pad" #n "\n\t"
#define TN "s_branch 3f\n\t"
#define TOUCH(w, tbl) asm volatile("s_getpc_b64 s[90:91]\n1:\n\ts_lshl2_add_u32 s92, %0, s90\n\ts_addc_u32 s93, s91, 0\n\ts_add_u32 s92, s92, 2f-1b\n\ts_addc_u32 s93, s93, 0\n\t" \
    "s_add_u32 s90, s90, 3f-1b\n\ts_addc_u32 s91, s91, 0\n\ts_setpc_b64 s[92:93]\n2:\n\t" tbl "3:" :: "s"(w) : "s90", "s91", "s92", "s93", "scc")

__global__ void __attribute__((aligned(4096))) __launch_bounds__(NTHR, 3) fused_decode(Params p) {
  __shared__ __attribute__((aligned(16))) char smem[L_BYTES];
  LAS char* L = (LAS char*)smem;
  const int tid = threadIdx.x, w = __builtin_amdgcn_readfirstlane(tid >> 6), l = tid & 63;
  const int c = blockIdx.x, x = c & 7, j = c >> 3;
  const int i16 = l & 15, g = l >> 4;
  TOUCH(w, TB(0) TB(13) TB(11) TB(12) TN TN TN TN TB(21) TN);
  const int xp = x >> 1, xo = x & 1, hi = j >> 4, sp = j & 15;
  const int pair = (2 * xo + hi) * 4 + xp;
  const int qd = j < 11 ? 0 : j < 22 ? 1 : 2, qi = 22 * qd + xo * (qd == 2 ? 10 : 11) + j - 11 * qd;
  const int qks = qi & 3, qcb = ((xp + 1 + qd) & 3) * 16 + (qi >> 2);
  const int r8 = l >> 3, ce = ((l & 7) ^ (r8 & 2)) * 16;
  const rsrc_t rws = mk(p.ws, WS_END);
  const rsrc_t rx = mk(p.x, (w & 12) == 4 ? 8192 : 0);
  const v4u xa_ = __builtin_amdgcn_raw_buffer_load_b128(rx, (w & 3) * 2048 + l * 16, 0, 0), xb_ = __builtin_amdgcn_raw_buffer_load_b128(rx, (w & 3) * 2048 + 1024 + l * 16, 0, 0);
  const unsigned ev = __builtin_amdgcn_raw_buffer_load_b32(rws, w == 8 ? WS_E : OOB, 0, AUX_SC1);
  int sq = qks * 256 * 8192 + qcb * 128;
  {
    int m = 0;
    asm volatile("s_cmp_lg_u32 %2, 9\n\ts_cbranch_scc1 2f\n\ts_setprio 3\n1:\n\ts_mov_b32 m0, %0\n\ts_addk_i32 %0, 0x1000\n\t"
                 "buffer_load_dwordx4 %3, %5, %1 offen" POL " lds\n\ts_add_u32 %1, %1, 0xfc00\n\t"
                 "buffer_load_dwordx4 %4, %5, %1 offen offset:1024" POL " lds\n\ts_add_u32 %1, %1, 0xfc00\n\t"
                 "buffer_load_dwordx4 %3, %5, %1 offen offset:2048" POL " lds\n\ts_add_u32 %1, %1, 0xfc00\n\t"
                 "buffer_load_dwordx4 %4, %5, %1 offen offset:3072" POL " lds\n\ts_add_u32 %1, %1, 0x10c00\n\ts_cmp_lg_u32 %0, 0x6000\n\ts_cbranch_scc1 1b\n2:"
                 : "+s"(m), "+s"(sq) : "s"(w), "v"(r8 * 8192 + ce), "v"((r8 * 8192 + ce) ^ 64), "s"(mk(p.wq, 8 << 20)) : "memory", "scc");
  }
  SB();
  const int kn = (xp + 1) & 3, rr64 = kn * 16 + (j >> 1), cq = 2 * (j & 1) + xo;
  const int d0 = 56 * rr64;
  const int kg = (xp + 3) & 3, ns = kg == 2 ? 1 : 2, sb0 = (11 - 14 * kg) & 7;
  const int js = xo ? 1 : 22, u = (unsigned)(j - js) < 4u ? 2 * (j - js) + xo : 8 + 2 * (j < js ? j : j - 4) + xo;
  const int v = u - 8 * ns, vb_ = v >> 2, vc = vb_ + (vb_ >= sb0), gl = u < 8 * ns ? sb0 + (u & 8) : vc + (vc >= sb0 + 8);
  const int ng = u < 8 * ns ? 1 : gl < 14 ? 2 : 0;
  const int gsrc = u < 8 * ns ? u & 7 : (v & 3) * 2, gcb = kg * 14 + gl;
  const int k_wo = 12 + 4 * ng, k_dn = k_wo + 4, ktot = k_dn + 4;

  {
    static_assert(WIN == 26, "progress = tasks issued - 3 behind the first instruction of a task");
    const int kb = g * 256 + ((i16 ^ g) * 16), vb = g * 256 + ((i16 ^ (2 * g)) * 16);
    const u16* pgu = (gsrc & 4) ? p.wu : p.wg;
    int a0, a1, a2, a3, a4_, ta, tb;
#define LD1(v) "s_mov_b32 m0, s76\n\ts_addk_i32 s76, 0x400\n\tbuffer_load_dwordx4 " v ", s[72:75], s78 offen" POL " lds\n\ts_add_u32 s78, s78, s77\n\ts_waitcnt vmcnt(" STR(WIN - 1) ")\n\t"
#define LBASE(ptr, bytes) "s_mov_b64 s[72:73], " ptr "\n\ts_and_b32 s73, s73, 0xffff\n\ts_mov_b32 s74, " bytes "\n\t"
#define LW(ld) "s_mov_b32 s77, 8*" #ld "\n\tv_mul_u32_u24 %[a0], " #ld ", %[r8]\n\tv_or_b32 %[a0], %[a0], %[ce]\n\tv_xor_b32 %[a1], 64, %[a0]\n\tv_mov_b32 %[a2], %[a0]\n\tv_mov_b32 %[a3], %[a1]\n\t"
#define LPAD "s_branch 4f\n.Lfd_pad21:\n\ts_setpc_b64 s[90:91]\n4:\n\t"
#define LCALL(i) "s_swappc_b64 s[82:83], s[70:71]\n\t"
#define LPUB(n) "s_waitcnt vmcnt(" #n "*8)\n\ts_sub_i32 s81, s79, " #n "\n\tv_mov_b32 %[tb], s81\n\tds_write_b32 %[ta], %[tb]\n\t"
    asm volatile(
      LPAD "s_cmp_lg_u32 %[w], 9\n\ts_cbranch_scc1 9f\n\t"
      "s_getpc_b64 s[70:71]\n\ts_add_u32 s70, s70, 5f-.\n\ts_addc_u32 s71, s71, 0\n\t"
      "s_mov_b32 s75, 0x20000\n\ts_movk_i32 s76, 0x6000\n\ts_mov_b32 s79, 3\n\tv_mov_b32 %[ta], %c[cnt]\n\ts_mov_b32 s80, 1\n\t"
      LBASE("%[wq]", "0x800000") LW(8192) "s_mov_b32 s78, %[sq]\n\t" LCALL(1) "s_barrier\n\t"
      LBASE("%[kc]", "0x2000000") "s_movk_i32 s77, " "0x4000" "\n\tv_mov_b32 %[a0], %[kb]\n\tv_xor_b32 %[a1], 64, %[kb]\n\tv_xor_b32 %[a2], 0x80, %[kb]\n\tv_xor_b32 %[a3], 0xc0, %[kb]\n\ts_mov_b32 s78, %[skv]\n\t" LCALL(2)
      LBASE("%[vc]", "0x2000000") "v_mov_b32 %[a0], %[vb]\n\tv_xor_b32 %[a1], 0x80, %[vb]\n\tv_mov_b32 %[a2], %[vb]\n\tv_mov_b32 %[a3], %[a1]\n\ts_mov_b32 s78, %[skv]\n\t" LCALL(3)
      "s_cmp_eq_u32 %[ng], 0\n\ts_cbranch_scc1 1f\n\t"
      LBASE("%[gu]", "0x700000") LW(7168) "s_mov_b32 s78, %[sgu]\n\t" LCALL(4) "s_cmp_eq_u32 %[ng], 1\n\ts_cbranch_scc1 1f\n\t" LCALL(5)
      "1:\n\t"
      LBASE("%[wo]", "0x800000") "s_movk_i32 s77, 0x1000\n\tv_mov_b32 %[a0], %[ob]\n\tv_xor_b32 %[a1], 64, %[ob]\n\tv_xor_b32 %[a2], 0x80, %[ob]\n\tv_xor_b32 %[a3], 0xc0, %[ob]\n\ts_mov_b32 s78, %[swo]\n\t" LCALL(6)
      LBASE("%[wd]", "%[dend]") "s_mov_b32 s78, %[sdn]\n\t" LCALL(7)
      "s_mov_b64 exec, 1\n\t"
      LPUB(3) LPUB(2) LPUB(1) LPUB(0)
      "s_mov_b64 exec, -1\n\t" "s_branch 9f\n\t"
      "5:\n\ts_sub_i32 s81, s79, %c[nu]\n\ts_cmp_lt_i32 s81, 0\n\ts_cbranch_scc1 8f\n\ts_and_b32 s84, s81, 3\n\ts_cbranch_scc1 8f\n\t"
      "v_add_u32 %[tb], s81, %[ta]\n\ts_mov_b32 s84, 0x100000\n"
      "7:\n\tds_read_b32 %[a4], %[tb] offset:%c[pj]\n\ts_waitcnt lgkmcnt(0)\n\tv_readfirstlane_b32 s85, %[a4]\n\ts_cmp_eq_u32 s85, 0x01010101\n\ts_cbranch_scc1 8f\n\ts_sub_u32 s84, s84, 1\n\ts_cbranch_scc0 7b\n"
      "8:\n\t"
      "s_sub_i32 s81, s79, (" STR(WIN) "-2)/8\n\ts_max_i32 s81, s81, 0\n\tv_mov_b32 %[tb], s81\n\t"
      LD1("%[a0]") "s_mov_b64 exec, 1\n\tds_write_b32 %[ta], %[tb]\n\ts_mov_b64 exec, -1\n\t"
      LD1("%[a1]") LD1("%[a2]") LD1("%[a3]") LD1("%[a0]") LD1("%[a1]") LD1("%[a2]") LD1("%[a3]")
      "s_cmp_eq_u32 s76, %c[ring]\n\ts_cselect_b32 s76, 0, s76\n\ts_add_u32 s79, s79, 1\n\ts_cmp_eq_u32 s79, %c[nu]+8\n\ts_cselect_b32 s76, 0x18000, s76\n\ts_sub_u32 s80, s80, 1\n\ts_cmp_lg_u32 s80, 0\n\ts_cbranch_scc1 5b\n\ts_mov_b32 s80, 4\n\ts_setpc_b64 s[82:83]\n"
      "9:"
      : [a0] "=&v"(a0), [a1] "=&v"(a1), [a2] "=&v"(a2), [a3] "=&v"(a3), [a4] "=&v"(a4_), [ta] "=&v"(ta), [tb] "=&v"(tb)
      : [w] "s"(w), [wq] "s"(p.wq), [kc] "s"(p.kc), [vc] "s"(p.vc), [gu] "s"(pgu), [wo] "s"(p.wo), [wd] "s"(p.wd),
        [sq] "s"(sq), [skv] "s"((pair * 8192 + 6144) * 256 + sp * 1024), [sgu] "s"((gsrc & 3) * 256 * 7168 + gcb * 128), [ng] "s"(ng),
        [swo] "s"(rr64 * 64 * 2048 + cq * 512), [sdn] "s"(d0 * 2048 + cq * 512), [dend] "s"((d0 + 56) * 2048),
        [r8] "v"(r8), [ce] "v"(ce), [kb] "v"(kb), [vb] "v"(vb), [ob] "v"((l >> 5) * 2048 + (((l & 31) ^ ((l >> 5) * 2)) * 16)),
        [cnt] "i"(L_CNT + C_PROG * 4), [nu] "i"(NUNIT), [pj] "i"((C_PJ - C_PROG) * 4), [ring] "i"(NUNIT * 8192)
      : "memory", "scc", "s70", "s71", "s72", "s73", "s74", "s75", "s76", "s77", "s78", "s79", "s80", "s81", "s82", "s83", "s84", "s85");
  }
  if (w != 9) {
  asm volatile("ds_write_b32 %2, %1\n\ts_mov_b64 exec, 0xffff\n\tds_write_b32 %0, %1\n\ts_mov_b64 exec, -1\n\ts_bitcmp1_b32 %3, 2\n\ts_cbranch_scc1 1f\n\ts_waitcnt lgkmcnt(0)\n\ts_barrier\n1:"
               :: "v"(L_CNT + l * 4), "v"(0), "v"((l < 32 ? L_OF : L_HF - 128) + 576 + l * 4), "s"(w) : "memory", "scc");
  SB();
  PAD(0);
  const unsigned tagv = ev + 1u;
  asm volatile("s_cmp_lg_u32 %0, 8\n\ts_cbranch_scc1 1f\n\ts_mov_b64 exec, 1\n\tds_write_b32 %1, %2\n\tds_write_b32 %1, %3 offset:8\n\ts_mov_b64 exec, -1\n1:"
               :: "s"(w), "v"(L_CNT + C_TAG * 4), "v"(tagv), "v"(1u) : "memory", "scc");
  static_assert(C_TAGV == C_TAG + 2, "offset above");
  unsigned prog = 0;
  if (w == 8) {
    PADA(5);
    TOUCH(0, TB(7));
    TOUCH(0, TB(6));
    TOUCH(0, TB(16));
    TOUCH(0, TB(17));
    TOUCH(0, TB(18));
    __builtin_amdgcn_s_setprio(1);
    const int oidx = xp * 1024 + (cq ^ 1) * 256 + (j >> 1) * 16 + (l & 3) * 4;
    const v2u xres = *(const v2u*)(p.x + oidx);
    const unsigned tag = __builtin_amdgcn_readfirstlane(tagv);
    const rsrc_t rout = mk(p.out, 8192);
    const bool live = i16 < 14;
    const bool slow = (((d0 + 4 * i16) >> 6) & 7) == 3;
    int hoff = live ? WS_GGU + (g * 1792 + d0 / 2 + i16 * 2) * 8 : OOB, hof2 = live && slow ? hoff + 4 * 1792 * 8 : OOB;
    int hw = L_HF + g * 144 + (i16 >> 3) * 64 + (i16 & 3) * 16 + ((i16 >> 2) & 1) * 8;
    int ooff = l < 4 ? oidx * 2 : OOB, off5 = WS_G5 + (((cq ^ 1) * 64 + xp * 16 + (j >> 1)) * 64) * 64 + l * 16;
    PINV(hoff); PINV(hof2); PINV(hw); PINV(ooff); PINV(off5);
    v4u H[4], H2[4];
    wait_pos(L, prog, TH, ktot);
    issue4(rws, hoff, 8 * 1792 * 8, H);
    issue4(rws, hof2, 8 * 1792 * 8, H2);
    PAD(7);
    check8(rws, hoff, hof2, 8 * 1792 * 8, live ? tag : 0u, live && slow ? tag : 0u, H, H2);
    {
#define SUM4(f, a, d) ((f(H[a][d]) + f(H2[a][d])) + (f(H[a + 1][d]) + f(H2[a + 1][d])))
      const unsigned g0 = pk(SUM4(bflo, 0, 0), SUM4(bfhi, 0, 0)), g1 = pk(SUM4(bflo, 0, 2), SUM4(bfhi, 0, 2));
      const unsigned u0 = pk(SUM4(bflo, 2, 0), SUM4(bfhi, 2, 0)), u1 = pk(SUM4(bflo, 2, 2), SUM4(bfhi, 2, 2));
      *(LAS v2u*)(L + hw) = v2u{pk(bflo(g0) * bflo(u0), bfhi(g0) * bfhi(u0)), pk(bflo(g1) * bflo(u1), bfhi(g1) * bfhi(u1))};
      set_word(C_HF, 1u);
    }
    PAD(6);
    wait_eq<1>(L, C_G5, ALL1);
    v4u R[4];
    issue4(rws, off5, 1024, R);
    check4(rws, off5, 1024, tag, R);
    float r0 = (bflo(R[0][0]) + bflo(R[1][0])) + (bflo(R[2][0]) + bflo(R[3][0])), r1 = (bfhi(R[0][0]) + bfhi(R[1][0])) + (bfhi(R[2][0]) + bfhi(R[3][0]));
    float r2 = (bflo(R[0][2]) + bflo(R[1][2])) + (bflo(R[2][2]) + bflo(R[3][2])), r3 = (bfhi(R[0][2]) + bfhi(R[1][2])) + (bfhi(R[2][2]) + bfhi(R[3][2]));
    r0 += dpp<ROR4>(r0); r1 += dpp<ROR4>(r1); r2 += dpp<ROR4>(r2); r3 += dpp<ROR4>(r3);
    r0 += dpp<ROR8>(r0); r1 += dpp<ROR8>(r1); r2 += dpp<ROR8>(r2); r3 += dpp<ROR8>(r3);
    r0 = xrow_sum(r0); r1 = xrow_sum(r1); r2 = xrow_sum(r2); r3 = xrow_sum(r3);
    __builtin_amdgcn_raw_buffer_store_b64(v2u{pk(bflo(xres[0]) + r0, bfhi(xres[0]) + r1), pk(bflo(xres[1]) + r2, bfhi(xres[1]) + r3)}, rout, ooff, 0, 0);
    stg1(rws, c == 0 && l == 0 ? WS_E : OOB, tag);
  } else {
    const int w4 = w & 3, q4 = i16 >> 2, pp = l & 3;
    const int wsw = 2 * (((q4 >> 1) & 1) | ((g & 1) << 1)), wrow = (8 * g + q4) * 128 + 8 * (pp & 1);
    f32x4 acc0 = Z4, acc1 = Z4, acc2 = Z4, acc3 = Z4;
    auto proj = [&](int k0, int kx) {
      const int wa = wrow + (((4 * (w & 1) + (pp >> 1)) ^ wsw) * 16);
      LAS char* xa = L + L_XN + pp * 2048 + (kx + g * 8) * 2;
      PADA(11);
      v4u xf[8];
#pragma unroll
      for (int s = 0; s < 8; ++s) xf[s] = *(LAS v4u*)(xa + s * 64);
#pragma unroll
      for (int t = 0; t < 4; ++t) {
        const int u = unit_of(k0 + t) + wa;
        if (t == 3) PAD(12);
        wait_prog<1>(L, prog, k0 + t + 1);
        const v4u f0 = trfrag(L + u, 512), f1 = trfrag(L + (u ^ 32), 512), f2 = trfrag(L + u + 4096, 512), f3 = trfrag(L + (u ^ 32) + 4096, 512);
        acc0 = mfma(f0, xf[2 * t], acc0); acc1 = mfma(f1, xf[2 * t], acc1);
        acc2 = mfma(f2, xf[2 * t + 1], acc2); acc3 = mfma(f3, xf[2 * t + 1], acc3);
      }
    };
    if (w < 4) {
      PADA(1);
      TOUCH(w, TB(14) TB(8) TB(2) TB(4));
      int qoff = WS_GQ + ((2 * xo + hi) * 4096 + xp * 1024 + w * 256 + l * 2) * 8;
      int qw = L_QS + 2 * w * 256 + (((l >> 2) ^ (4 * w)) * 16) + (l & 3) * 4;
      int qr = L_QS + (i16 & 7) * 256 + ((g ^ (2 * (i16 & 7))) * 16);
      int ka = (4 + w) * 8192 + i16 * 256 + ((g ^ i16) * 16);
      PINV(qoff); PINV(qw); PINV(qr); PINV(ka);
      wait_eq<2>(L, C_TAGV, 1u);
      const unsigned tag = get_word(L, C_TAG);
      wait_prog<2>(L, prog, TQ);
      wait_eq<1>(L, C_PJ, ALL1);
      v4u Q[4], Q2[4];
      issue4(rws, qoff, 4 * 4096 * 8, Q);
      issue4(rws, qoff + 1024, 4 * 4096 * 8, Q2);
      wait_prog<1>(L, prog, 5 + w);
      v4u KF[8];
#pragma unroll
      for (int n = 0; n < 8; ++n) KF[n] = *(LAS v4u*)(L + ((ka ^ ((n & 3) * 64)) + (n >> 2) * 4096));
      set_byte(C_KD, w, 1u);
      const int rr = 4 * g + q4, va = 8 * 8192 + rr * 256 + (((4 * w + (pp >> 1)) ^ (2 * (rr & 7))) * 16) + 8 * (pp & 1);
      v4u VF[8];
#pragma unroll
      for (int t = 0; t < 4; ++t) { if (t == 2) PAD(14); wait_prog<1>(L, prog, 9 + t); VF[2 * t] = trfrag(L + va + t * 8192, 4096); VF[2 * t + 1] = trfrag(L + (va ^ 32) + t * 8192, 4096); }
      int goff = WS_G2 + ((pair * 16 + 2 * (i16 & 7) + (w >> 1)) * 16 + sp) * 256 + (w & 1) * 128 + (i16 >> 3) * 64 + g * 16;
      int loff = tid < 8 ? WS_G2L + ((pair * 8 + l) * 16 + sp) * 8 : OOB;
      PINV(goff); PINV(loff);
#pragma unroll
      for (int t = 0; t < 8; ++t) PINV(VF[t]);
      __builtin_amdgcn_s_setprio(2);
      SB();
      check8(rws, qoff, qoff + 1024, 4 * 4096 * 8, tag, tag, Q, Q2);
      PAD(8);
#define QSUM(q, d) ((asf(q[0][d]) + asf(q[1][d])) + (asf(q[2][d]) + asf(q[3][d])))
      *(LAS unsigned*)(L + qw) = pk(QSUM(Q, 0), QSUM(Q, 2));
      *(LAS unsigned*)(L + (qw ^ 32) + 256) = pk(QSUM(Q2, 0), QSUM(Q2, 2));
      set_byte(C_QS, w, 1u);
      wait_eq<0>(L, C_QS, ALL1);
      v4u qf[4];
#pragma unroll
      for (int s = 0; s < 4; ++s) qf[s] = *(LAS v4u*)(L + (qr ^ (s * 64)));
      SB();
      f32x4 d0 = Z4, d1 = Z4;
#pragma unroll
      for (int s = 0; s < 4; ++s) { d0 = mfma(KF[s], qf[s], d0); d1 = mfma(KF[4 + s], qf[s], d1); }
      float sc[8] = {d0[0], d0[1], d0[2], d0[3], d1[0], d1[1], d1[2], d1[3]};
#pragma unroll
      for (int n = 0; n < 8; n += 2) {
        const unsigned a = pk(sc[n], sc[n + 1]);
        const unsigned b = pk(bflo(a) * 0.08838834764831845f, bfhi(a) * 0.08838834764831845f);
        sc[n] = bflo(b); sc[n + 1] = bfhi(b);
      }
      float m = __builtin_fmaxf(__builtin_fmaxf(__builtin_fmaxf(sc[0], sc[1]), sc[2]), __builtin_fmaxf(__builtin_fmaxf(sc[3], sc[4]), sc[5]));
      m = __builtin_fmaxf(__builtin_fmaxf(m, sc[6]), sc[7]);
      m = xrow_max(m) * 1.4426950408889634f;
#pragma unroll
      for (int n = 0; n < 8; ++n) sc[n] = __builtin_amdgcn_exp2f(fmaf(sc[n], 1.4426950408889634f, -m));
      *(LAS v4u*)(L + L_PS + w * 1024 + l * 16) = v4u{pk(sc[0], sc[1]), pk(sc[2], sc[3]), pk(sc[4], sc[5]), pk(sc[6], sc[7])};
      const float ls = xrow_sum(((sc[0] + sc[1]) + (sc[2] + sc[3])) + ((sc[4] + sc[5]) + (sc[6] + sc[7])));
      *(LAS f2_t*)(L + L_ML + i16 * 32 + w * 8) = f2_t{m, ls};
      set_byte(C_P, w, 1u);
      PAD(2);
      {
        const bool up = i16 & 8;
        wait_eq<0>(L, C_P, ALL1);
        const f32x4 ml0 = *(LAS f32x4*)(L + L_ML + i16 * 32), ml1 = *(LAS f32x4*)(L + L_ML + i16 * 32 + 16);
        v4u PF[4];
#pragma unroll
        for (int t = 0; t < 4; ++t) PF[t] = *(LAS v4u*)(L + L_PS + t * 1024 + l * 16);
        SB();
        f32x4 A[8];
#pragma unroll
        for (int t = 0; t < 8; ++t) A[t] = mfma(VF[t], PF[t >> 1], Z4);
        const float M = vmax(vmax(ml0[0], ml0[2]), vmax(ml1[0], ml1[2]));
        const float b0 = __builtin_amdgcn_exp2f(ml0[0] - M), b1 = __builtin_amdgcn_exp2f(ml0[2] - M), b2 = __builtin_amdgcn_exp2f(ml1[0] - M), b3 = __builtin_amdgcn_exp2f(ml1[2] - M);
        const float Ls = (b0 * ml0[1] + b1 * ml0[3]) + (b2 * ml1[1] + b3 * ml1[3]);
        const float inv = __builtin_amdgcn_rcpf(Ls);
        const f32x4 ov = (((up ? A[1] : A[0]) * b0 + (up ? A[3] : A[2]) * b1) + ((up ? A[5] : A[4]) * b2 + (up ? A[7] : A[6]) * b3)) * inv;
        stg(rws, goff, v4u{pk(ov[0], ov[1]), tag, pk(ov[2], ov[3]), tag});
        stg2(rws, loff, v2u{asu(M + __builtin_amdgcn_logf(Ls)), tag});
      }
      PAD(4);
      int off2 = WS_G2 + ((w * 4 + kn) * 16 + (j >> 1)) * 4096 + l * 16, offl = WS_G2L + (((w * 4 + kn) * 8 + (j >> 2)) * 16 + pp * 4 + g) * 8;
      int ow = L_OF + w * 144 + (i16 >> 3) * 64 + (i16 & 3) * 16 + ((i16 >> 2) & 1) * 8;
      int wa = rr * 512 + (((8 * w + (pp >> 1)) ^ (2 * (rr & 7))) * 16) + 8 * (pp & 1);
      int soff = WS_G5 + ((cq * 64 + pp * 16 + 4 * w + q4) * 64 + rr64) * 64 + g * 16;
      PINV(off2); PINV(offl); PINV(ow); PINV(wa); PINV(soff);
      wait_pos(L, prog, T2G, ktot);
      v4u P[4];
      v2u S = ldg2(rws, offl);
      issue4(rws, off2, 1024, P);
      auto frags = [&](int k0, int s, v4u* F) {
        LAS char* u0 = L + unit_of(k0 + 2 * s), * u1 = L + unit_of(k0 + 2 * s + 1);
#pragma unroll
        for (int cbk = 0; cbk < 4; ++cbk) F[cbk] = trfrag2(u0 + (wa ^ (cbk * 32)), u1 + (wa ^ (cbk * 32)));
      };
      int aa[4], da[8];
#pragma unroll
      for (int cbk = 0; cbk < 4; ++cbk) {
        aa[cbk] = L_OF + (q4 == cbk ? pp : 4) * 144 + g * 16; PINV(aa[cbk]);
        da[2 * cbk] = unit_of(k_dn + 2) + (wa ^ (cbk * 32)); da[2 * cbk + 1] = unit_of(k_dn + 3) + (wa ^ (cbk * 32)); PINV(da[2 * cbk]); PINV(da[2 * cbk + 1]);
      }
      SB();
      PADA(16);
      v4u WF[8];
      wait_prog<1>(L, prog, k_wo + 2); frags(k_wo, 0, WF);
      wait_prog<1>(L, prog, k_wo + 4); frags(k_wo, 1, WF + 4);
#pragma unroll
      for (int t = 0; t < 8; ++t) PINV(WF[t]);
      SB();
      {
#pragma unroll 1
        for (int it = 0; it < SPIN_G; ++it) {
          const bool ok = tagged(P[0], tag) & tagged(P[1], tag) & tagged(P[2], tag) & tagged(P[3], tag) & (S[1] == tag);
          if (__all(ok)) break;
          CBAR();
          if (!ok) { S = ldg2(rws, offl); issue4(rws, off2, 1024, P); }
        }
      }
      {
        float M = asf(S[0]);
        PADV(17, M);
        M = vmax(M, dpp<XOR1>(M)); M = vmax(M, dpp<XOR2>(M)); M = xrow_max(M);
        const float e = __builtin_amdgcn_exp2f(asf(S[0]) - M);
        const float a[4] = {dpp<0x00>(e), dpp<0x55>(e), dpp<0xAA>(e), dpp<0xFF>(e)};
        const float ws = (a[0] + a[1]) + (a[2] + a[3]);
        float o0 = 0.f, o1 = 0.f, o2 = 0.f, o3 = 0.f;
#pragma unroll
        for (int t = 0; t < 4; ++t) { o0 = fmaf(a[t], bflo(P[t][0]), o0); o1 = fmaf(a[t], bfhi(P[t][0]), o1); o2 = fmaf(a[t], bflo(P[t][2]), o2); o3 = fmaf(a[t], bfhi(P[t][2]), o3); }
        const float inv = __builtin_amdgcn_rcpf(xrow_sum(ws));
        o0 = xrow_sum(o0) * inv; o1 = xrow_sum(o1) * inv; o2 = xrow_sum(o2) * inv; o3 = xrow_sum(o3) * inv;
        *(LAS v2u*)(L + ow) = v2u{pk(o0, o1), pk(o2, o3)};
        set_byte(C_OF, w, 1u);
      }
      {
        auto act = [&](int cbk, int off) { return *(LAS v4u*)(L + aa[cbk] + off); };
        f32x4 ro = Z4, rd0 = Z4;
        v4u a[8], DF[8];
        wait_eq<0>(L, C_OF, ALL1);
#pragma unroll
        for (int cbk = 0; cbk < 4; ++cbk) { a[cbk] = act(cbk, 0); a[4 + cbk] = act(cbk, 64); }
        SB();
#pragma unroll
        for (int cbk = 0; cbk < 4; ++cbk) { ro = mfma(WF[cbk], a[cbk], ro); rd0 = mfma(WF[4 + cbk], a[4 + cbk], rd0); }
        wait_prog<0>(L, prog, k_dn + 2); frags(k_dn, 0, DF);
        PAD(18);
        wait_prog<0>(L, prog, ktot);
#pragma unroll
        for (int cbk = 0; cbk < 4; ++cbk) DF[4 + cbk] = trfrag2(L + da[2 * cbk], L + da[2 * cbk + 1]);
        SB();
        wait_eq<0>(L, C_HF, 1u);
#pragma unroll
        for (int cbk = 0; cbk < 4; ++cbk) { a[cbk] = act(cbk, L_HF - L_OF); a[4 + cbk] = act(cbk, L_HF - L_OF + 64); }
        SB();
#pragma unroll
        for (int cbk = 0; cbk < 4; ++cbk) { ro = mfma(DF[cbk], a[cbk], ro); rd0 = mfma(DF[4 + cbk], a[4 + cbk], rd0); }
        const f32x4 r = ro + rd0;
        stg(rws, soff, v4u{pk(r[0], r[1]), tag, pk(r[2], r[3]), tag});
        set_byte(C_G5, w, 1u);
      }
    } else {
      float v[16];
#pragma unroll
      for (int n = 0; n < 4; ++n) { v[2 * n] = bflo(xa_[n]); v[2 * n + 1] = bfhi(xa_[n]); v[8 + 2 * n] = bflo(xb_[n]); v[9 + 2 * n] = bfhi(xb_[n]); }
      float s = 0.f;
#pragma unroll
      for (int n = 0; n < 16; n += 4) s += (v[n] + v[n + 1]) + (v[n + 2] + v[n + 3]);
      const float mean = wave_sum(s) * (1.f / 1024.f);
      float q0 = 0.f, q1 = 0.f;
#pragma unroll
      for (int n = 0; n < 16; n += 2) { const float d0 = v[n] - mean, d1 = v[n + 1] - mean; q0 = fmaf(d0, d0, q0); q1 = fmaf(d1, d1, q1); }
      const float var = rbf(wave_sum(q0 + q1) * (1.f / 1024.f));
      const float mb_ = rbf(mean);
      float rs = __builtin_amdgcn_rcpf(rbf(__builtin_amdgcn_sqrtf(rbf(var + 1e-5f))));
      PADV(13, rs);
      v4u oa, ob;
#pragma unroll
      for (int n = 0; n < 4; ++n) {
        const unsigned da = pk(v[2 * n] - mb_, v[2 * n + 1] - mb_), db = pk(v[8 + 2 * n] - mb_, v[9 + 2 * n] - mb_);
        oa[n] = pk(bflo(da) * rs, bfhi(da) * rs); ob[n] = pk(bflo(db) * rs, bfhi(db) * rs);
      }
      *(LAS v4u*)(L + L_XN + w4 * 2048 + l * 16) = oa;
      *(LAS v4u*)(L + L_XN + w4 * 2048 + 1024 + l * 16) = ob;
      BARRIER();
      set_byte(C_XN, w4, 1u);
      wait_eq<1>(L, C_TAGV, 1u);
      const unsigned tag = get_word(L, C_TAG);
      __builtin_amdgcn_s_setprio(1);
      if (w < 6) {
        wait_eq<0>(L, C_XN, ALL1);
#pragma unroll 1
        for (int job = 0; job <= ng; ++job) {
          int soff = job ? (i16 >= 8 ? OOB : WS_GGU + ((gsrc * 4 + pp) * 1792 + gcb * 32) * 8 + (w & 1) * 128 + q4 * 64 + g * 16)
                         : WS_GQ + ((qks * 4 + pp) * 4096 + qcb * 64) * 8 + (w & 1) * 256 + (q4 & 1) * 128 + g * 32 + (q4 >> 1) * 16;
          PINV(soff);
          proj(job ? 8 + 4 * job : 0, job ? ((gsrc & 3) + job - 1) * 256 : qks * 256);
          if (job == 1 && ng == 2) asm volatile("s_mov_b64 exec, 1\n\tds_write_b16 %0, %1\n\ts_mov_b64 exec, -1" :: "v"(L_CNT + C_GD * 4 + (w & 1) * 2), "v"(0x0101u) : "memory");
          else {
            const f32x4 r = (i16 & 4) ? acc1 + acc3 : acc0 + acc2;
            const bool u2 = i16 & 8;
            stg(rws, soff, v4u{job ? pk(r[0], r[1]) : asu(u2 ? r[2] : r[0]), tag, job ? pk(r[2], r[3]) : asu(u2 ? r[3] : r[1]), tag});
            asm volatile("s_mov_b64 exec, 1\n\tds_write_b16 %0, %1\n\ts_mov_b64 exec, -1" :: "v"(L_CNT + C_PJ * 4 + (w & 1) * 2), "v"(0x0101u) : "memory");
            acc0 = Z4; acc1 = Z4; acc2 = Z4; acc3 = Z4;
          }
        }
      }
    }
  }
  }
}

extern "C" void kernel_launch(void* const* d_in, const int* in_sizes, int n_in, void* d_out, int out_size, void* d_ws, size_t ws_size, hipStream_t stream) {
  Params p;
  p.x = (const u16*)d_in[0]; p.kc = (const u16*)d_in[1]; p.vc = (const u16*)d_in[2]; p.wq = (const u16*)d_in[3]; p.wo = (const u16*)d_in[4];
  p.wg = (const u16*)d_in[6]; p.wu = (const u16*)d_in[7]; p.wd = (const u16*)d_in[8]; p.out = (u16*)d_out; p.ws = (char*)d_ws;
  (void)in_sizes; (void)n_in; (void)out_size; (void)ws_size;
  fused_decode<<<256, NTHR, 0, stream>>>(p);
}
```
